```python
import torch

def _softmax(x, axis=-1):
    return torch.softmax(x, dim=axis)
def _gelu(x):
    return torch.nn.functional.gelu(x, approximate="tanh")
def _silu(x):
    return torch.nn.functional.silu(x)
def _sigmoid(x):
    return torch.sigmoid(x)
def _layernorm(x, axis=-1, eps=1e-5):
    m = x.mean(dim=axis, keepdim=True)
    v = x.var(dim=axis, keepdim=True, unbiased=False)
    return (x - m) / torch.sqrt(v + eps)
def _scatter_add(tgt, idx, src, axis=0):
    return tgt.clone().index_add_(axis, idx, src)
def _segment_sum(src, seg, n):
    out = torch.zeros((n,) + src.shape[1:], dtype=src.dtype)
    return out.index_add_(0, seg, src)
def _pad(x, pw):
    flat = tuple(v for pair in reversed(pw) for v in pair)
    return torch.nn.functional.pad(x, flat)

kernel_name = 'parallel_sum__ssm_conv_s4__mixer_channel_mlp__prompt'

def setup_inputs():

    _w = (lambda a: (lambda t: t.to(torch.bfloat16) if t.is_floating_point() else t)(torch.as_tensor(a)))
    def _A_setup():
        B, L, D, N = 1, 2048, 448, 256
        A_re = -(lambda _x: torch.exp(torch.as_tensor(_x)))((lambda *s: torch.randn(*s, dtype=torch.bfloat16) * 3.2)(D, N))
        A_im = torch.pi * (lambda *s: torch.randn(*s, dtype=torch.bfloat16) * 0.05)(D, N)
        B_re = (lambda *s: torch.randn(*s, dtype=torch.bfloat16) * 0.1)(D, N)
        B_im = (lambda *s: torch.randn(*s, dtype=torch.bfloat16) * 0.1)(D, N)
        C_re = (lambda *s: torch.randn(*s, dtype=torch.bfloat16) * 0.1)(D, N)
        C_im = (lambda *s: torch.randn(*s, dtype=torch.bfloat16) * 0.1)(D, N)
        log_dt = (lambda *s: torch.randn(*s, dtype=torch.bfloat16) * 0.05)(D) * 0.5 - 4.0
        Dskip = (lambda *s: torch.randn(*s, dtype=torch.bfloat16) * 0.05)(D)
        return {
            "u": _w((lambda *s: torch.randn(*s, dtype=torch.bfloat16) * 0.05)(B, L, D)),
            "A_re": _w(A_re),
            "A_im": _w(A_im),
            "B_re": _w(B_re),
            "B_im": _w(B_im),
            "C_re": _w(C_re),
            "C_im": _w(C_im),
            "log_dt": _w(log_dt),
            "Dskip": _w(Dskip),
        }
    _A_in = _A_setup()

    def _B_setup():
        B, S, D, F = 1, 2048, 448, 1792
        return {
            "x": _w((lambda *s: torch.randn(*s, dtype=torch.bfloat16) * 0.05)(B, S, D)),
            "w1": _w((lambda *s: torch.randn(*s, dtype=torch.bfloat16) * 3.2)(D, F) * (D ** -0.5)),
            "b1": _w((lambda *s: torch.randn(*s, dtype=torch.bfloat16) * 0.8)(F)),
            "w2": _w((lambda *s: torch.randn(*s, dtype=torch.bfloat16) * 0.05)(F, D) * (F ** -0.5)),
            "b2": _w((lambda *s: torch.randn(*s, dtype=torch.bfloat16) * 0.05)(D)),
        }
    _B_in = _B_setup()

    return {
        "A_u": _A_in["u"],
        "A_A_re": _A_in["A_re"],
        "A_A_im": _A_in["A_im"],
        "A_B_re": _A_in["B_re"],
        "A_B_im": _A_in["B_im"],
        "A_C_re": _A_in["C_re"],
        "A_C_im": _A_in["C_im"],
        "A_log_dt": _A_in["log_dt"],
        "A_Dskip": _A_in["Dskip"],
        "B_x": _B_in["x"],
        "B_w1": _B_in["w1"],
        "B_b1": _B_in["b1"],
        "B_w2": _B_in["w2"],
        "B_b2": _B_in["b2"],
    }

def reference(A_u, A_A_re, A_A_im, A_B_re, A_B_im, A_C_re, A_C_im, A_log_dt, A_Dskip, B_x, B_w1, B_b1, B_w2, B_b2):
    def _A_ref(u, A_re, A_im, B_re, B_im, C_re, C_im, log_dt, Dskip):
        Bsz, L, D = u.shape
        dt = torch.exp(log_dt)[:, None]
        A = A_re + 1j * A_im
        Bc = B_re + 1j * B_im
        Cc = C_re + 1j * C_im
        logdA = dt * A
        dB = dt * Bc
        l = (lambda x, dt: x.to(dt))(torch.arange(L), A_re.dtype)
        P = torch.exp(logdA[:, :, None] * l[None, None, :])
        K = torch.einsum("dn,dnl,dn->dl", Cc, P, dB).real
        L2 = 2 * L
        Kp = _pad(K, ((0, 0), (0, L)))
        up = (lambda x, *d: x.permute(*d))(u, 0, 2, 1)
        up = _pad(up, ((0, 0), (0, 0), (0, L)))
        Kf = (lambda x, axes=(-2,-1): torch.fft.rfft2(x.float(), dim=axes))(Kp[:, None, :], axes=(-2, -1))
        Uf = (lambda x, axes=(-2,-1): torch.fft.rfft2(x.float(), dim=axes))(up[:, :, None, :], axes=(-2, -1))
        Yf = Uf * Kf[None, :, :, :]
        y = (lambda x, s, axes=(-2,-1): torch.fft.irfft2(x, s=s, dim=axes).to(torch.bfloat16))(Yf, (1, L2), axes=(-2, -1))[:, :, 0, :L]
        y = (lambda x, *d: x.permute(*d))(y, 0, 2, 1) + u * Dskip[None, None, :]
        return y

    def _B_ref(x, w1, b1, w2, b2):
        y = _layernorm(x, axis=-1)
        h = _gelu(torch.einsum("bsd,df->bsf", y, w1) + b1)
        return torch.einsum("bsf,fd->bsd", h, w2) + b2

    x = A_u
    xn = _layernorm(x, axis=-1)
    return x + _A_ref(xn, A_A_re, A_A_im, A_B_re, A_B_im, A_C_re, A_C_im, A_log_dt, A_Dskip) + _B_ref(xn, B_w1, B_b1, B_w2, B_b2)
```

```cpp
#include <hip/hip_runtime.h>
#include <hip/hip_bf16.h>
#include <stdint.h>

constexpr int L = 2048, D = 448, N = 256, F = 1792;

__device__ __forceinline__ float bf2f(unsigned short v) { return __uint_as_float(((unsigned)v) << 16); }
__device__ __forceinline__ unsigned short f2bf(float f) {
    __hip_bfloat16 b = __float2bfloat16(f);
    return *reinterpret_cast<unsigned short*>(&b);
}
__device__ __forceinline__ float rbf(float f) { return bf2f(f2bf(f)); }
__device__ __forceinline__ float wsum(float v) {
    for (int o = 32; o > 0; o >>= 1) v += __shfl_xor(v, o);
    return v;
}

__global__ void ln_kernel(const unsigned short* x, unsigned short* xn, unsigned short* y2) {
    int t = blockIdx.x * 4 + (threadIdx.x >> 6);
    int lane = threadIdx.x & 63;
    float v[7];
    float s = 0;
    for (int i = 0; i < 7; i++) { v[i] = bf2f(x[t * D + lane + 64 * i]); s += v[i]; }
    float m = wsum(s) / D;
    float q = 0;
    for (int i = 0; i < 7; i++) { float d = v[i] - m; q += d * d; }
    float var = rbf(wsum(q) / D);
    float mb = rbf(m);
    float sd = rbf(sqrtf(rbf(var + 1e-5f)));
    s = 0;
    for (int i = 0; i < 7; i++) { v[i] = rbf(rbf(v[i] - mb) / sd); xn[t * D + lane + 64 * i] = f2bf(v[i]); s += v[i]; }
    m = wsum(s) / D;
    q = 0;
    for (int i = 0; i < 7; i++) { float d = v[i] - m; q += d * d; }
    var = rbf(wsum(q) / D);
    mb = rbf(m);
    sd = rbf(sqrtf(rbf(var + 1e-5f)));
    for (int i = 0; i < 7; i++) { y2[t * D + lane + 64 * i] = f2bf(rbf(v[i] - mb) / sd); }
}

__global__ void kgen_kernel(const unsigned short* Are, const unsigned short* Aim, const unsigned short* Bre,
                            const unsigned short* Bim, const unsigned short* Cre, const unsigned short* Cim,
                            const unsigned short* logdt, float* K) {
    int d = blockIdx.x;
    __shared__ float zr[N], zi[N], wr[N], wi[N];
    float dt = rbf(expf(bf2f(logdt[d])));
    for (int n = threadIdx.x; n < N; n += blockDim.x) {
        zr[n] = dt * bf2f(Are[d * N + n]);
        zi[n] = dt * bf2f(Aim[d * N + n]);
        float br = dt * bf2f(Bre[d * N + n]), bi = dt * bf2f(Bim[d * N + n]);
        float cr = bf2f(Cre[d * N + n]), ci = bf2f(Cim[d * N + n]);
        wr[n] = cr * br - ci * bi;
        wi[n] = cr * bi + ci * br;
    }
    __syncthreads();
    for (int l = threadIdx.x; l < L; l += blockDim.x) {
        float lf = rbf((float)l);
        float acc = 0;
        for (int n = 0; n < N; n++) {
            float e = expf(zr[n] * lf);
            float sn, cs;
            sincosf(zi[n] * lf, &sn, &cs);
            acc += e * (wr[n] * cs - wi[n] * sn);
        }
        K[d * L + l] = acc;
    }
}

__global__ void conv_kernel(const float* K, const unsigned short* xn, float* yc) {
    int d = blockIdx.x;
    __shared__ float ks[L], us[L];
    for (int i = threadIdx.x; i < L; i += blockDim.x) { ks[i] = K[d * L + i]; us[i] = bf2f(xn[i * D + d]); }
    __syncthreads();
    for (int t = threadIdx.x; t < L; t += blockDim.x) {
        float acc = 0;
        for (int s = 0; s <= t; s++) acc += ks[t - s] * us[s];
        yc[t * D + d] = acc;
    }
}

__global__ void gemm1_kernel(const unsigned short* y2, const unsigned short* w1, const unsigned short* b1, unsigned short* h) {
    int t = blockIdx.x;
    __shared__ float ys[D];
    for (int i = threadIdx.x; i < D; i += blockDim.x) ys[i] = bf2f(y2[t * D + i]);
    __syncthreads();
    for (int f = threadIdx.x; f < F; f += blockDim.x) {
        float acc = 0;
        for (int k = 0; k < D; k++) acc += ys[k] * bf2f(w1[k * F + f]);
        float a = rbf(rbf(acc) + bf2f(b1[f]));
        float g = 0.5f * a * (1.f + tanhf(0.7978845608028654f * (a + 0.044715f * a * a * a)));
        h[t * F + f] = f2bf(g);
    }
}

__global__ void gemm2_kernel(const unsigned short* h, const unsigned short* w2, const unsigned short* b2,
                             const unsigned short* x, const unsigned short* xn, const float* yc,
                             const unsigned short* dskip, unsigned short* out) {
    int t = blockIdx.x;
    __shared__ float hs[F];
    for (int i = threadIdx.x; i < F; i += blockDim.x) hs[i] = bf2f(h[t * F + i]);
    __syncthreads();
    for (int d = threadIdx.x; d < D; d += blockDim.x) {
        float acc = 0;
        for (int k = 0; k < F; k++) acc += hs[k] * bf2f(w2[k * D + d]);
        float mlp = rbf(rbf(acc) + bf2f(b2[d]));
        float a = rbf(rbf(yc[t * D + d]) + rbf(bf2f(xn[t * D + d]) * bf2f(dskip[d])));
        float o = rbf(bf2f(x[t * D + d]) + a) + mlp;
        out[t * D + d] = f2bf(o);
    }
}

extern "C" void kernel_launch(void* const* d_in, const int* in_sizes, int n_in, void* d_out, int out_size,
                              void* d_ws, size_t ws_size, hipStream_t stream) {
    const unsigned short* x = (const unsigned short*)d_in[0];
    char* ws = (char*)d_ws;
    unsigned short* xn = (unsigned short*)ws;            ws += L * D * 2;
    unsigned short* y2 = (unsigned short*)ws;            ws += L * D * 2;
    float* K = (float*)ws;                               ws += L * D * 4;
    float* yc = (float*)ws;                              ws += L * D * 4;
    unsigned short* h = (unsigned short*)ws;             ws += L * F * 2;
    ln_kernel<<<L / 4, 256, 0, stream>>>(x, xn, y2);
    kgen_kernel<<<D, 256, 0, stream>>>((const unsigned short*)d_in[1], (const unsigned short*)d_in[2],
                                       (const unsigned short*)d_in[3], (const unsigned short*)d_in[4],
                                       (const unsigned short*)d_in[5], (const unsigned short*)d_in[6],
                                       (const unsigned short*)d_in[7], K);
    conv_kernel<<<D, 256, 0, stream>>>(K, xn, yc);
    gemm1_kernel<<<L, 256, 0, stream>>>(y2, (const unsigned short*)d_in[10], (const unsigned short*)d_in[11], h);
    gemm2_kernel<<<L, 256, 0, stream>>>(h, (const unsigned short*)d_in[12], (const unsigned short*)d_in[13], x, xn, yc,
                                        (const unsigned short*)d_in[8], (unsigned short*)d_out);
}
```

```cpp
#include <hip/hip_runtime.h>
#include <stdint.h>

typedef unsigned v4u __attribute__((ext_vector_type(4)));
typedef unsigned v2u __attribute__((ext_vector_type(2)));
typedef float f32x4 __attribute__((ext_vector_type(4)));
typedef float f32x2 __attribute__((ext_vector_type(2)));
typedef __bf16 bf16x8 __attribute__((ext_vector_type(8)));
typedef _Float16 f16x8 __attribute__((ext_vector_type(8)));
typedef __bf16 bf16x2 __attribute__((ext_vector_type(2)));
typedef _Float16 f16x2 __attribute__((ext_vector_type(2)));
typedef short s16x4 __attribute__((ext_vector_type(4)));
typedef unsigned long long u64;
typedef unsigned short u16;
#define GAS __attribute__((address_space(1)))
#define LAS __attribute__((address_space(3)))
#define RLX_AGENT __ATOMIC_RELAXED, __HIP_MEMORY_SCOPE_AGENT
#define DEV __device__ __forceinline__

constexpr int L_ = 2048, D_ = 448, N_ = 256, F_ = 1792;
constexpr unsigned WS_E = 0, WS_FW = 1024, WS_FL = 2048, WS_F2 = 4096, WS_F3 = 8192, WS_DBG = 16384;
constexpr unsigned WS_XNT = 1u << 20;
constexpr unsigned WS_W2T = WS_XNT + D_ * L_ * 2;
constexpr unsigned WS_HF = WS_W2T + F_ * D_ * 2;
constexpr unsigned WS_AP = WS_HF + L_ * F_ * 2;
constexpr unsigned WS_END = WS_AP + D_ * L_ * 2;
constexpr int LD_W1 = 0;
constexpr int LD_SCR = 114688;
constexpr int LD_KT = 151552;
constexpr int LD_CS = 154368;
constexpr int LD_B1 = 155392;
constexpr int LD_MISC = 155904;
constexpr int LD_TOTAL = 156160;
constexpr int TZ_CS = 4160, TZ_CH = 8 * TZ_CS;
constexpr int LD_TZ = 0, LD_U = 2 * TZ_CH;
constexpr int LD_CP = LD_U + 2 * 4608;
constexpr int LD_AT = 131072;
constexpr unsigned SPIN_CAP = 1u << 18;

struct Params {
    const u16 *x, *are, *aim, *bre, *bim, *cre, *cim, *logdt, *dskip, *w1, *b1, *w2, *b2;
    u16* out;
    char* ws;
};

DEV float bf2f(unsigned v) { return __uint_as_float(v << 16); }
DEV float bflo(unsigned v) { return __uint_as_float(v << 16); }
DEV float bfhi(unsigned v) { return __uint_as_float(v & 0xffff0000u); }
DEV unsigned pkbf(float a, float b) { f32x2 t = {a, b}; return __builtin_bit_cast(unsigned, __builtin_convertvector(t, bf16x2)); }
DEV unsigned pkh(float a, float b) { f32x2 t = {a, b}; return __builtin_bit_cast(unsigned, __builtin_convertvector(t, f16x2)); }
DEV float rbf(float a) { return bflo(pkbf(a, a)); }
DEV f32x4 mfma_bf(v4u a, v4u b, f32x4 c) { return __builtin_amdgcn_mfma_f32_16x16x32_bf16(__builtin_bit_cast(bf16x8, a), __builtin_bit_cast(bf16x8, b), c, 0, 0, 0); }
DEV f32x4 mfma_h(v4u a, v4u b, f32x4 c) { return __builtin_amdgcn_mfma_f32_16x16x32_f16(__builtin_bit_cast(f16x8, a), __builtin_bit_cast(f16x8, b), c, 0, 0, 0); }
DEV __amdgpu_buffer_rsrc_t mkrs(const void* p, unsigned bytes) { return __builtin_amdgcn_make_buffer_rsrc((void*)p, 0, bytes, 0x00020000); }
DEV void st16_sc1(__amdgpu_buffer_rsrc_t rs, unsigned off, v4u v) { __builtin_amdgcn_raw_buffer_store_b128(v, rs, off, 0, 16); }
DEV void bar() { asm volatile("s_waitcnt lgkmcnt(0)" ::: "memory"); __builtin_amdgcn_s_barrier(); asm volatile("" ::: "memory"); }
DEV void sb() { __builtin_amdgcn_sched_barrier(0); }
template <int CTRL> DEV unsigned dppu(unsigned x) { return (unsigned)__builtin_amdgcn_mov_dpp((int)x, CTRL, 0xf, 0xf, true); }
template <int CTRL> DEV float dppf(float x) { return __uint_as_float(dppu<CTRL>(__float_as_uint(x))); }
DEV float rowsum16(float v) { v += dppf<0xB1>(v); v += dppf<0x4E>(v); v += dppf<0x124>(v); v += dppf<0x128>(v); return v; }
DEV float rowmax16(float v) { v = fmaxf(v, dppf<0xB1>(v)); v = fmaxf(v, dppf<0x4E>(v)); v = fmaxf(v, dppf<0x124>(v)); v = fmaxf(v, dppf<0x128>(v)); return v; }
DEV float wavesum(float v) { v = rowsum16(v); v += __shfl_xor(v, 16); v += __shfl_xor(v, 32); return v; }
DEV float wavemax(float v) { v = rowmax16(v); v = fmaxf(v, __shfl_xor(v, 16)); v = fmaxf(v, __shfl_xor(v, 32)); return v; }
DEV v4u ldsr128(const char* lds, int off) { return *(const LAS v4u*)(LAS char*)(lds + off); }
template <class T> DEV T ldsr(const char* lds, int off) { return *(const T*)(lds + off); }
template <class T> DEV void ldsw(char* lds, int off, T v) { *(T*)(lds + off) = v; }

DEV void qtr(v4u& L0, v4u& L1, v4u& L2, v4u& L3, bool b0, bool b1) {
#pragma unroll
    for (int c = 0; c < 4; c++) {
        unsigned d1 = dppu<0xA0>(L1[c]), d0 = dppu<0xF5>(L0[c]), d3 = dppu<0xA0>(L3[c]), d2 = dppu<0xF5>(L2[c]);
        unsigned a0 = b0 ? d1 : L0[c], a1 = b0 ? L1[c] : d0, a2 = b0 ? d3 : L2[c], a3 = b0 ? L3[c] : d2;
        unsigned e2 = dppu<0x44>(a2), e0 = dppu<0xEE>(a0), e3 = dppu<0x44>(a3), e1 = dppu<0xEE>(a1);
        L0[c] = b1 ? e2 : a0; L2[c] = b1 ? a2 : e0; L1[c] = b1 ? e3 : a1; L3[c] = b1 ? a3 : e1;
    }
}

DEV void wait_flags(const char* ws, unsigned off, int cnt, u64 nonce, int lane) {
    const GAS u64* f = (const GAS u64*)(ws + off) + (lane < cnt ? lane : 0);
    for (unsigned spins = 0; spins < SPIN_CAP; ++spins) {
        u64 v = __hip_atomic_load(f, RLX_AGENT);
        if (__all(v == nonce)) break;
        __builtin_amdgcn_s_sleep(1);
    }
    asm volatile("" ::: "memory");
}
DEV void set_flag(char* ws, unsigned off, u64 nonce) { __hip_atomic_store((GAS u64*)(ws + off), nonce, RLX_AGENT); }

#define STAMP(i) do {} while (0)

struct cf { float x, y; };
DEV cf cmul(cf a, cf b) { return {a.x * b.x - a.y * b.y, a.x * b.y + a.y * b.x}; }
DEV cf cexp_l(float zr2, float zrev, float l) {
    float e = __builtin_amdgcn_exp2f(zr2 * l);
    float r = __builtin_amdgcn_fractf(zrev * l);
    return {e * __builtin_amdgcn_cosf(r), e * __builtin_amdgcn_sinf(r)};
}

DEV void aux_block(const Params& p, char* lds, int a) {
    const int tid = threadIdx.x, w = __builtin_amdgcn_readfirstlane(tid >> 6), l = tid & 63;
    const int r = l & 15, g = l >> 4;
    char* ws = p.ws;
    STAMP(0);
    const u64 nonce = __hip_atomic_load((const GAS u64*)(ws + WS_E), RLX_AGENT) + 1;
    const auto rs_w2 = mkrs(p.w2, F_ * D_ * 2);
    const auto rs_x = mkrs(p.x, L_ * D_ * 2);
    const auto rs_ws = mkrs(ws, WS_END);
    const int u0 = a * 8 + w;
    const bool two = u0 + 256 < 392;
    v2u wv[2][8];
#pragma unroll
    for (int k = 0; k < 2; k++) {
        int u = u0 + 256 * k; if (u >= 392) u = u0;
        const int ks = u / 7, j2 = u % 7;
#pragma unroll
        for (int j = 0; j < 8; j++)
            wv[k][j] = __builtin_amdgcn_raw_buffer_load_b64(rs_w2, (4 * g) * 896 + r * 8, (32 * ks + 16 * (j >> 2) + (j & 3)) * 896 + j2 * 128, 0);
    }
    v4u xv[2][4];
    const int row0 = 64 * a + 8 * w + 2 * g;
#pragma unroll
    for (int k = 0; k < 2; k++)
#pragma unroll
        for (int i = 0; i < 4; i++) {
            unsigned vo = (row0 + k) * 896 + (i < 3 ? r : (r & 7)) * 16;
            xv[k][i] = __builtin_amdgcn_raw_buffer_load_b128(rs_x, vo, i * 256, 0);
        }
    sb();
#pragma unroll
    for (int k = 0; k < 2; k++) {
        if (k == 1 && !two) break;
        const int u = u0 + 256 * k;
#pragma unroll
        for (int e = 0; e < 4; e++) {
            v4u o;
#pragma unroll
            for (int i = 0; i < 4; i++) {
                unsigned lo = wv[k][2 * i][e >> 1], hi = wv[k][2 * i + 1][e >> 1];
                o[i] = __builtin_amdgcn_perm(hi, lo, (e & 1) ? 0x07060302u : 0x05040100u);
            }
            st16_sc1(rs_ws, l * 16 + WS_W2T + (u * 4 + e) * 1024, o);
        }
    }
    asm volatile("s_waitcnt vmcnt(0)" ::: "memory");
    bar();
    if (tid == 0) set_flag(ws, WS_FW + a * 8, nonce);
    STAMP(1);
    const bool v3 = r < 8;
    unsigned xh[2][16];
#pragma unroll
    for (int k = 0; k < 2; k++) {
        float v[32];
#pragma unroll
        for (int i = 0; i < 4; i++)
#pragma unroll
            for (int c = 0; c < 4; c++) { v[i * 8 + 2 * c] = bflo(xv[k][i][c]); v[i * 8 + 2 * c + 1] = bfhi(xv[k][i][c]); }
        float s = 0.f, s3 = 0.f;
#pragma unroll
        for (int i = 0; i < 24; i++) s += v[i];
#pragma unroll
        for (int i = 24; i < 32; i++) s3 += v[i];
        s += v3 ? s3 : 0.f;
        const float mean = rowsum16(s) * (1.f / 448.f);
        float q = 0.f, q3 = 0.f;
#pragma unroll
        for (int i = 0; i < 24; i++) { float d = v[i] - mean; q += d * d; }
#pragma unroll
        for (int i = 24; i < 32; i++) { float d = v[i] - mean; q3 += d * d; }
        q += v3 ? q3 : 0.f;
        const float var = rbf(rowsum16(q) * (1.f / 448.f));
        const float mb = rbf(mean);
        const float sd = rbf(__builtin_sqrtf(rbf(var + 1e-5f)));
        const float rs = 1.f / sd;
#pragma unroll
        for (int i = 0; i < 16; i++) {
            unsigned d = pkbf(v[2 * i] - mb, v[2 * i + 1] - mb);
            unsigned n = pkbf(bflo(d) * rs, bfhi(d) * rs);
            xh[k][i] = pkh(bflo(n), bfhi(n));
        }
    }
    {
        const int tau = 4 * w + g;
        const int base = r * 8 * 128 + ((tau ^ ((r & 7) << 2)) << 2);
#pragma unroll
        for (int i = 0; i < 4; i++) {
            if (i == 3 && !v3) break;
#pragma unroll
            for (int c = 0; c < 4; c++) {
                unsigned a0 = xh[0][i * 4 + c], a1 = xh[1][i * 4 + c];
                ldsw<unsigned>(lds, base + (i * 128 + 2 * c) * 128, __builtin_amdgcn_perm(a1, a0, 0x05040100u));
                ldsw<unsigned>(lds, base + (i * 128 + 2 * c + 1) * 128, __builtin_amdgcn_perm(a1, a0, 0x07060302u));
            }
        }
    }
    bar();
    {
        const int u = tid & 7, d0 = tid >> 3;
#pragma unroll
        for (int i = 0; i < 7; i++) {
            const int d = d0 + 64 * i;
            v4u o = ldsr<v4u>(lds, d * 128 + ((u ^ ((d >> 3) & 7)) << 4));
            st16_sc1(rs_ws, d * 4096 + u * 16 + WS_XNT + a * 128, o);
        }
    }
    asm volatile("s_waitcnt vmcnt(0)" ::: "memory");
    bar();
    if (tid == 0) set_flag(ws, WS_FL + a * 8, nonce);
    STAMP(2);
    if (a == 0 && w == 0) {
        wait_flags(ws, WS_FW, 32, nonce, l);
        const GAS u64* f = (const GAS u64*)(ws + WS_F2);
        for (unsigned spins = 0; spins < SPIN_CAP; ++spins) {
            bool ok = true;
#pragma unroll
            for (int k = 0; k < 4; k++) { int idx = l + 64 * k; u64 v = __hip_atomic_load(f + idx, RLX_AGENT); ok &= ((idx & 15) >= 14) || v == nonce; }
            if (__all(ok)) break;
            __builtin_amdgcn_s_sleep(4);
        }
        if (l == 0) set_flag(ws, WS_E, nonce);
    }
}

template <bool CS, int S0, int S1>
DEV void gemm1_stage(const char* lds, const int (&ad)[8], int delta3, const v4u (&xf)[14], f32x4 (&acc)[8], f32x4 (&cs)[2]) {
    const v4u ones = {0x3F803F80u, 0x3F803F80u, 0x3F803F80u, 0x3F803F80u};
#pragma unroll
    for (int S = S0; S < S1; S++) {
#pragma unroll
        for (int q = 0; q < (S < 3 ? 4 : 2); q++) {
            const int roff = (128 * S + 8 * q) * 256;
#pragma unroll
            for (int n = 0; n < 8; n++) {
                const int o = ad[n] + (S == 3 ? delta3 : 0) + roff;
                s16x4 lo = __builtin_amdgcn_ds_read_tr16_b64_v4i16((LAS s16x4*)(LAS char*)(lds + o));
                s16x4 hi = __builtin_amdgcn_ds_read_tr16_b64_v4i16((LAS s16x4*)(LAS char*)(lds + o + 1024));
                v2u a = __builtin_bit_cast(v2u, lo), b = __builtin_bit_cast(v2u, hi);
                v4u A = {a[0], a[1], b[0], b[1]};
                acc[n] = mfma_bf(A, xf[4 * S + q], acc[n]);
                if (CS && n < 2) cs[n] = mfma_bf(A, ones, cs[n]);
            }
        }
    }
}

template <bool X>
DEV void conv_part(const char* lds, int abase, int bbase, f32x4 (&acc)[5]) {
    constexpr int NA = X ? 8 : 7;
#pragma unroll
    for (int c = 0; c < 4; c++) {
        v4u A[NA], B[NA];
#pragma unroll
        for (int i = 0; i < NA; i++) {
            A[i] = ldsr<v4u>(lds, abase - c * 64 - i * 512);
            B[i] = ldsr<v4u>(lds, bbase - c * 64 + i * 512);
        }
#pragma unroll
        for (int t = 0; t < (X ? 5 : 3); t++) {
            const int n = X ? (t < 4 ? t : 7) : 4 + t;
#pragma unroll
            for (int i = 0; i <= n; i++) acc[t] = mfma_h(A[i], B[n - i], acc[t]);
        }
    }
}

DEV void main_block(const Params& p, char* lds, int b) {
    const int tid = threadIdx.x, w = __builtin_amdgcn_readfirstlane(tid >> 6), l = tid & 63;
    const int r16 = l & 15, g = l >> 4;
    char* ws = p.ws;
    const int g8 = b & 7, rr = (b - 32) >> 3;
    const int i1 = 4 * (g8 >> 1) + rr / 7, j1 = 7 * (g8 & 1) + rr % 7;
    const int i2 = 4 * g8 + rr / 7, j2 = rr % 7;
    const int q = 28 * g8 + rr;
    const int ch = w >> 2, dch = 2 * q + ch;
    STAMP(0);
    const auto rs_x = mkrs(p.x, L_ * D_ * 2);
    const auto rs_w1 = mkrs(p.w1, D_ * F_ * 2);
    const auto rs_ws = mkrs(ws, WS_END);
    const int pn = dch * N_ + 64 * (w & 3) + l;
    const unsigned p_are = p.are[pn], p_aim = p.aim[pn], p_bre = p.bre[pn], p_bim = p.bim[pn], p_cre = p.cre[pn], p_cim = p.cim[pn];
    const unsigned p_ldt = p.logdt[dch];
    u64 ev = 0;
    if (tid == 0) ev = __hip_atomic_load((const GAS u64*)(ws + WS_E), RLX_AGENT);
    unsigned b1v = 0;
    if (tid < 128) b1v = p.b1[128 * j1 + tid];
    sb();
    v4u xf[16];
    {
        const unsigned vo = (128 * i1 + 16 * w + (l & 12)) * 896 + g * 64 + (l & 3) * 16;
#pragma unroll
        for (int S = 0; S < 3; S++)
#pragma unroll
            for (int j = 0; j < 4; j++) xf[4 * S + j] = __builtin_amdgcn_raw_buffer_load_b128(rs_x, vo, j * 896 + S * 256, 0);
        const unsigned vo3 = g < 2 ? vo : 0x80000000u;
#pragma unroll
        for (int j = 0; j < 4; j++) xf[12 + j] = __builtin_amdgcn_raw_buffer_load_b128(rs_x, vo3, j * 896 + 768, 0);
    }
    v4u wv[14];
    {
        const unsigned vo = (4 * w + g) * 3584 + j1 * 256 + r16 * 16;
#pragma unroll
        for (int i = 0; i < 14; i++) wv[i] = __builtin_amdgcn_raw_buffer_load_b128(rs_w1, vo, i * 32 * 3584, 0);
    }
    sb();
    {
        char* scr = lds + LD_SCR + w * 4608;
        const float dt = rbf(expf(bf2f(p_ldt)));
        float zr = dt * bf2f(p_are);
        const float zi = dt * bf2f(p_aim);
        const float br = dt * bf2f(p_bre), bi = dt * bf2f(p_bim), cr = bf2f(p_cre), ci = bf2f(p_cim);
        cf W = {cr * br - ci * bi, cr * bi + ci * br};
        const float shift = fmaxf(wavemax(zr), 0.f);
        zr -= shift;
        const float wmax = wavemax(fmaxf(fabsf(W.x), fabsf(W.y)));
        const float wsum = wavesum(fabsf(W.x) + fabsf(W.y));
        int we = __builtin_amdgcn_frexp_expf(wmax);
        we = min(max(we, -100), 100);
        const float wsc = __builtin_amdgcn_ldexpf(1.f, 14 - we);
        W.x *= wsc; W.y *= wsc;
        if (l == 0) { f32x4 info = {__builtin_amdgcn_ldexpf(1.f, we - 14), wsum, shift, 0.f}; ldsw<f32x4>(lds, LD_MISC + 16 + w * 16, info); }
        const float zr2 = zr * 1.4426950408889634f, zrev = zi * 0.15915494309189535f;
        const cf E1 = cexp_l(zr2, zrev, 1.f), E8 = cexp_l(zr2, zrev, 8.f), E256 = cexp_l(zr2, zrev, 256.f);
        v4u fl[2][4], fh[2][4];
        const int rd = r16 * 288 + g * 16;
        cf P[4];
        P[0] = {1.f, 0.f}; P[1] = E8; P[2] = cmul(E8, E8); P[3] = cmul(P[2], E8);
        const cf E32 = cmul(P[2], P[2]);
#pragma unroll
        for (int t = 0; t < 2; t++) {
#pragma unroll
            for (int k = 0; k < 4; k++) {
#pragma unroll
                for (int i = 0; i < 4; i++) {
                    ldsw<unsigned>(scr, (4 * k + i) * 288 + l * 4, pkh(P[i].x, P[i].y));
                    if (t == 0 || k < 3) P[i] = cmul(P[i], E32);
                }
            }
#pragma unroll
            for (int k = 0; k < 4; k++) fl[t][k] = ldsr<v4u>(scr, rd + k * 64);
        }
        const cf E2 = cmul(E1, E1), E4 = cmul(E2, E2);
        cf H[8];
        H[0] = W; H[1] = cmul(W, E1); H[2] = cmul(W, E2); H[3] = cmul(H[1], E2);
#pragma unroll
        for (int i = 0; i < 4; i++) H[4 + i] = cmul(H[i], E4);
#pragma unroll
        for (int i = 0; i < 8; i++) ldsw<unsigned>(scr, i * 288 + l * 4, pkh(H[i].x, -H[i].y));
        cf G = cmul(W, E256);
        { cf a = cmul(G, E2), c4 = cmul(G, E4), c6 = cmul(a, E4);
          ldsw<unsigned>(scr, 8 * 288 + l * 4, pkh(G.x, -G.y)); ldsw<unsigned>(scr, 9 * 288 + l * 4, pkh(a.x, -a.y));
          ldsw<unsigned>(scr, 10 * 288 + l * 4, pkh(c4.x, -c4.y)); ldsw<unsigned>(scr, 11 * 288 + l * 4, pkh(c6.x, -c6.y)); }
        G = cmul(G, E256);
        { cf a = cmul(G, E4); ldsw<unsigned>(scr, 12 * 288 + l * 4, pkh(G.x, -G.y)); ldsw<unsigned>(scr, 13 * 288 + l * 4, pkh(a.x, -a.y)); }
        G = cmul(G, E256);
        { cf a = cmul(G, E4); ldsw<unsigned>(scr, 14 * 288 + l * 4, pkh(G.x, -G.y)); ldsw<unsigned>(scr, 15 * 288 + l * 4, pkh(a.x, -a.y)); }
#pragma unroll
        for (int k = 0; k < 4; k++) fh[0][k] = ldsr<v4u>(scr, rd + k * 64);
#pragma unroll
        for (int i = 0; i < 5; i++) { G = cmul(G, E256); ldsw<unsigned>(scr, i * 288 + l * 4, pkh(G.x, -G.y)); }
#pragma unroll
        for (int k = 0; k < 4; k++) fh[1][k] = ldsr<v4u>(scr, rd + k * 64);
        f32x4 T[2][2];
#pragma unroll
        for (int th = 0; th < 2; th++)
#pragma unroll
            for (int tl = 0; tl < 2; tl++) {
                f32x4 a = {0.f, 0.f, 0.f, 0.f};
#pragma unroll
                for (int k = 0; k < 4; k++) a = mfma_h(fh[th][k], fl[tl][k], a);
                T[th][tl] = a;
            }
#pragma unroll
        for (int tl = 0; tl < 2; tl++) {
            const int lo = 16 * tl + r16;
            const int i0 = g < 2 ? 4 * g + 8 * lo : (g == 2 ? 256 + 4 * lo : 384 + 2 * lo);
            const int i1_ = i0 + (g == 3 ? 64 : 2);
            f32x2 u0 = {T[0][tl][0], T[0][tl][1]}, u1 = {T[0][tl][2], T[0][tl][3]};
            ldsw<f32x2>(scr, i0 * 4, u0); ldsw<f32x2>(scr, i1_ * 4, u1);
            if (g == 0) {
#pragma unroll
                for (int i = 0; i < 4; i++) ldsw<float>(scr, (512 + 32 * i + lo) * 4, T[1][tl][i]);
            }
        }
        if (l == 16) ldsw<float>(scr, 640 * 4, T[1][0][0]);
    }
    if (tid == 0) ldsw<u64>(lds, LD_MISC, ev + 1);
    if (tid < 128) ldsw<float>(lds, LD_B1 + tid * 4, bf2f(b1v));
    STAMP(1);
    float Rs, RM;
    {
        const bool b0 = l & 1, b1 = l & 2;
#pragma unroll
        for (int S = 0; S < 4; S++) qtr(xf[4 * S], xf[4 * S + 1], xf[4 * S + 2], xf[4 * S + 3], b0, b1);
#pragma unroll
        for (int c = 0; c < 4; c++) {
            auto s0 = __builtin_amdgcn_permlane32_swap(xf[12][c], xf[14][c], false, false); xf[12][c] = s0[0];
            auto s1 = __builtin_amdgcn_permlane32_swap(xf[13][c], xf[15][c], false, false); xf[13][c] = s1[0];
        }
        const v4u ones = {0x3F803F80u, 0x3F803F80u, 0x3F803F80u, 0x3F803F80u};
        f32x4 as = {0.f, 0.f, 0.f, 0.f}, aq = {0.f, 0.f, 0.f, 0.f};
#pragma unroll
        for (int k = 0; k < 14; k++) { as = mfma_bf(ones, xf[k], as); aq = mfma_bf(xf[k], xf[k], aq); }
        const float dq = (l & 2) ? ((l & 1) ? aq[3] : aq[2]) : ((l & 1) ? aq[1] : aq[0]);
        const float sq = __shfl(dq, r16 + 16 * (r16 >> 2));
        const float mean = as[0] * (1.f / 448.f);
        const float var = fmaxf(sq * (1.f / 448.f) - mean * mean, 0.f);
        const float m1 = rbf(mean);
        const float s1 = rbf(__builtin_sqrtf(rbf(rbf(var) + 1e-5f)));
        const float r1 = 1.f / s1;
        const float m2 = rbf((mean - m1) * r1);
        const float s2 = rbf(__builtin_sqrtf(rbf(rbf(var * r1 * r1) + 1e-5f)));
        Rs = r1 / s2;
        RM = Rs * (m1 + m2 * s1);
    }
    STAMP(2);
    const int wrow = 4 * w + g;
    const int wadr = wrow * 256 + (r16 & 1) * 16;
    auto w1_store = [&](int i) { const int f = (g & 3) | ((i & 1) << 2); ldsw<v4u>(lds, LD_W1 + i * 8192 + wadr + (((r16 >> 1) ^ f) << 5), wv[i]); };
#pragma unroll
    for (int i = 0; i < 8; i++) w1_store(i);
    int ad[8];
    {
        const int qq = r16 >> 2, pp = r16 & 3;
        const int fp = (qq | ((g & 1) << 2)) ^ (w & 6);
        const int base = LD_W1 + (32 * g + qq) * 256 + pp * 8;
#pragma unroll
        for (int n = 0; n < 8; n++) ad[n] = base + ((n ^ fp) << 5);
    }
    const int delta3 = (g >= 2) ? (16 - 64) * 256 : 0;
    f32x4 acc[8], cs[2];
#pragma unroll
    for (int n = 0; n < 8; n++) acc[n] = f32x4{0.f, 0.f, 0.f, 0.f};
    cs[0] = cs[1] = f32x4{0.f, 0.f, 0.f, 0.f};
    bar();
    STAMP(3);
    const u64 nonce = ldsr<u64>(lds, LD_MISC);
    float kinv;
    {
        const int t8 = tid & 255;
        f32x4 inf[4];
#pragma unroll
        for (int k = 0; k < 4; k++) inf[k] = ldsr<f32x4>(lds, LD_MISC + 16 + (4 * ch + k) * 16);
        const bool grow = (inf[0][2] + inf[1][2] + inf[2][2] + inf[3][2]) > 0.f;
        float bound = 0.f;
#pragma unroll
        for (int k = 0; k < 4; k++) bound += grow ? inf[k][1] * expf(inf[k][2] * 2048.f) : inf[k][1];
        int be = __builtin_amdgcn_frexp_expf(fminf(bound, 1e38f));
        be = min(max(be, -100), 120);
        const float ksc = __builtin_amdgcn_ldexpf(1.f, 15 - be);
        kinv = __builtin_amdgcn_ldexpf(1.f, be - 15);
#pragma unroll
        for (int k3 = 0; k3 < 3; k3++) {
            const int idx = t8 + 256 * k3;
            if (idx <= 640) {
                float s = 0.f;
                if (!grow) {
#pragma unroll
                    for (int k = 0; k < 4; k++) s += ldsr<float>(lds, LD_SCR + (4 * ch + k) * 4608 + idx * 4) * inf[k][0];
                } else {
                    const int rg = idx < 256 ? 0 : min((idx >> 7) - 1, 3);
                    const float lb = (float)((idx - 128 * rg) << rg);
#pragma unroll
                    for (int k = 0; k < 4; k++) s += ldsr<float>(lds, LD_SCR + (4 * ch + k) * 4608 + idx * 4) * inf[k][0] * expf(inf[k][2] * lb);
                }
                ldsw<_Float16>(lds, LD_KT + ch * 1296 + idx * 2, (_Float16)(s * ksc));
            }
        }
    }
    const v4u(&xf14)[14] = reinterpret_cast<const v4u(&)[14]>(xf);
    if (w & 1) gemm1_stage<false, 0, 1>(lds, ad, delta3, xf14, acc, cs); else gemm1_stage<true, 0, 1>(lds, ad, delta3, xf14, acc, cs);
#pragma unroll
    for (int i = 8; i < 12; i++) w1_store(i);
    if (w & 1) gemm1_stage<false, 1, 2>(lds, ad, delta3, xf14, acc, cs); else gemm1_stage<true, 1, 2>(lds, ad, delta3, xf14, acc, cs);
    w1_store(12); w1_store(13);
    bar();
    STAMP(4);
    unsigned TD[8];
    {
        const int v = tid & 255;
        unsigned kv[16];
#pragma unroll
        for (int hf = 0; hf < 2; hf++) {
            const int top = 2047 - 8 * v - 8 * hf;
            const int rg = top >= 1024 ? 3 : (top >= 512 ? 2 : (top >= 256 ? 1 : 0));
            const int cst = rg ? (1 << (rg - 1)) - 1 : 0, msk = rg ? 1 : 0;
#pragma unroll
            for (int e = 0; e < 8; e++) {
                const int ll = top - e;
                const int idx = 128 * rg + ((ll + cst + ((ll >> rg) & msk)) >> rg);
                unsigned val = ldsr<u16>(lds, LD_KT + ch * 1296 + (top < 0 ? 0 : idx) * 2);
                kv[8 * hf + e] = top < 0 ? 0u : val;
            }
        }
#pragma unroll
        for (int k = 0; k < 8; k++) TD[k] = kv[2 * k] | (kv[2 * k + 1] << 16);
    }
    if (w & 1) gemm1_stage<true, 2, 4>(lds, ad, delta3, xf14, acc, cs); else gemm1_stage<false, 2, 4>(lds, ad, delta3, xf14, acc, cs);
    if (r16 == 0) {
#pragma unroll
        for (int n = 0; n < 2; n++) ldsw<f32x4>(lds, LD_CS + (w & 1) * 512 + ((n ^ (w & 6)) * 16 + 4 * g) * 4, cs[n]);
    }
    wait_flags(ws, WS_FW, 32, nonce, l);
    wait_flags(ws, WS_FL, 32, nonce, l);
    bar();
    STAMP(5);
    const v4u uv = __builtin_amdgcn_raw_buffer_load_b128(rs_ws, l * 16 + (w & 3) * 1024, WS_XNT + dch * 4096, 16);
    v4u w2f[7][4];
    const int rot = (j2 + 2 * (i2 & 3)) % 7;
    auto ksof = [&](int i) { int k = i + rot; return 7 * w + (k >= 7 ? k - 7 : k); };
#pragma unroll
    for (int i = 0; i < 3; i++)
#pragma unroll
        for (int e = 0; e < 4; e++) w2f[i][e] = __builtin_amdgcn_raw_buffer_load_b128(rs_ws, l * 16, WS_W2T + ((ksof(i) * 7 + j2) * 4 + e) * 1024, 16);
    {
        const int v = tid & 255;
        const int base = LD_TZ + ch * TZ_CH + v * 16;
        unsigned O[7];
#pragma unroll
        for (int k = 0; k < 7; k++) O[k] = __builtin_amdgcn_alignbit(TD[k + 1], TD[k], 16);
#pragma unroll
        for (int s = 0; s < 4; s++) {
            ldsw<v4u>(lds, base + (2 * s) * TZ_CS, v4u{TD[s], TD[s + 1], TD[s + 2], TD[s + 3]});
            ldsw<v4u>(lds, base + (2 * s + 1) * TZ_CS, v4u{O[s], O[s + 1], O[s + 2], O[s + 3]});
        }
        const v4u z = {0u, 0u, 0u, 0u};
        if (v < 8) ldsw<v4u>(lds, LD_TZ + ch * TZ_CH + v * TZ_CS + 4096, z);
        if (v >= 64 && v < 96) ldsw<v4u>(lds, LD_U + ch * 4608 + (v - 64) * 16, z);
    }
    {
        const unsigned hb = WS_HF + ((8 * i1 + w) * 56 + 4 * j1) * 1024;
#pragma unroll
        for (int s = 0; s < 4; s++) {
            v4u o;
#pragma unroll
            for (int hf = 0; hf < 2; hf++) {
                const int n = 2 * s + hf;
                const int fo = ((n ^ (w & 6)) * 16 + 4 * g) * 4;
                const f32x4 bb = ldsr<f32x4>(lds, LD_B1 + fo), c0 = ldsr<f32x4>(lds, LD_CS + fo), c1 = ldsr<f32x4>(lds, LD_CS + 512 + fo);
                float gv[4];
#pragma unroll
                for (int i = 0; i < 4; i++) {
                    const float a = acc[n][i] * Rs + (bb[i] - RM * (c0[i] + c1[i]));
                    const float e = __builtin_amdgcn_exp2f(a * (-2.302208198f + -0.1029432397f * a * a));
                    gv[i] = a * __builtin_amdgcn_rcpf(1.f + e);
                }
                o[2 * hf] = pkbf(gv[0], gv[1]); o[2 * hf + 1] = pkbf(gv[2], gv[3]);
            }
            st16_sc1(rs_ws, l * 16 + hb + (s ^ ((w & 6) >> 1)) * 1024, o);
        }
    }
    sb();
#pragma unroll
    for (int i = 3; i < 7; i++)
#pragma unroll
        for (int e = 0; e < 4; e++) w2f[i][e] = __builtin_amdgcn_raw_buffer_load_b128(rs_ws, l * 16, WS_W2T + ((ksof(i) * 7 + j2) * 4 + e) * 1024, 16);
    sb();
    ldsw<v4u>(lds, LD_U + ch * 4608 + 512 + (w & 3) * 1024 + l * 16, uv);
    asm volatile("s_waitcnt vmcnt(16)" ::: "memory");
    bar();
    if (tid == 0) set_flag(ws, WS_F2 + (i1 * 16 + j1) * 8, nonce);
    STAMP(6);
    {
        const int half = (w >> 1) & 1;
        const int abase = LD_TZ + ch * TZ_CH + (7 - (r16 & 7)) * TZ_CS + (2024 - 128 * half - 8 * (r16 >> 3) + 8 * g) * 2;
        const int bbase = LD_U + ch * 4608 + (256 + 16 * r16 - 128 * half - 16 + 8 * g) * 2;
        f32x4 ca[5];
#pragma unroll
        for (int t = 0; t < 5; t++) ca[t] = f32x4{0.f, 0.f, 0.f, 0.f};
        const int pb = LD_CP + (ch * 2 + half) * 8192 + (4 * r16 + (g ^ ((r16 >> 1) & 3))) * 16;
        if (w & 1) {
            conv_part<false>(lds, abase, bbase, ca);
#pragma unroll
            for (int t = 0; t < 3; t++) ldsw<f32x4>(lds, pb + (4 + t) * 1024, ca[t]);
        } else {
            conv_part<true>(lds, abase, bbase, ca);
#pragma unroll
            for (int t = 0; t < 5; t++) ldsw<f32x4>(lds, pb + (t < 4 ? t : 7) * 1024, ca[t]);
        }
        if (l == 0 && (w & 3) == 0) ldsw<float>(lds, LD_MISC + 8 + ch * 4, kinv);
    }
    bar();
    STAMP(7);
    {
        const int cidx = tid;
        const int ap = (cidx >> 2) & 15;
        const int po = (cidx & ~3) * 16 + (((cidx & 3) ^ ((ap >> 1) & 3)) << 4);
        float av[2][4];
#pragma unroll
        for (int c = 0; c < 2; c++) {
            const f32x4 p0 = ldsr<f32x4>(lds, LD_CP + (c * 2) * 8192 + po), p1 = ldsr<f32x4>(lds, LD_CP + (c * 2 + 1) * 8192 + po);
            const v2u uu = ldsr<v2u>(lds, LD_U + c * 4608 + 512 + cidx * 8);
            const float ki = ldsr<float>(lds, LD_MISC + 8 + c * 4);
            const float ds = bf2f(p.dskip[2 * q + c]);
            const unsigned uu0 = uu[0], uu1 = uu[1];
            const f16x2 ua = __builtin_bit_cast(f16x2, uu0), ub = __builtin_bit_cast(f16x2, uu1);
            const float uf[4] = {(float)ua[0], (float)ua[1], (float)ub[0], (float)ub[1]};
#pragma unroll
            for (int i = 0; i < 4; i++) av[c][i] = rbf((p0[i] + p1[i]) * ki) + rbf(uf[i] * ds);
        }
        v4u o;
#pragma unroll
        for (int i = 0; i < 4; i++) o[i] = pkbf(av[0][i], av[1][i]);
        st16_sc1(rs_ws, tid * 16 + WS_AP + q * 8192, o);
    }
    asm volatile("s_waitcnt vmcnt(0)" ::: "memory");
    bar();
    if (tid == 0) set_flag(ws, WS_F3 + q * 8, nonce);
    STAMP(8);
    {
        const int jlo = (224 * w) >> 7, jhi = (224 * w + 223) >> 7;
        wait_flags(ws, WS_F2 + ((i2 >> 1) * 16 + jlo) * 8, jhi - jlo + 1, nonce, l);
    }
    STAMP(9);
    f32x4 oc[4][4];
#pragma unroll
    for (int m = 0; m < 4; m++)
#pragma unroll
        for (int e = 0; e < 4; e++) oc[m][e] = f32x4{0.f, 0.f, 0.f, 0.f};
    {
        v4u hf[7][4];
        const unsigned hb = WS_HF + (4 * i2) * 56 * 1024;
#pragma unroll
        for (int i = 0; i < 4; i++)
#pragma unroll
            for (int m = 0; m < 4; m++) hf[i][m] = __builtin_amdgcn_raw_buffer_load_b128(rs_ws, l * 16 + m * 56 * 1024, hb + ksof(i) * 1024, 16);
        sb();
#pragma unroll
        for (int i = 0; i < 7; i++) {
#pragma unroll
            for (int m = 0; m < 4; m++)
#pragma unroll
                for (int e = 0; e < 4; e++) oc[m][e] = mfma_bf(w2f[i][e], hf[i][m], oc[m][e]);
            if (i + 4 < 7) {
#pragma unroll
                for (int m = 0; m < 4; m++) hf[i + 4][m] = __builtin_amdgcn_raw_buffer_load_b128(rs_ws, l * 16 + m * 56 * 1024, hb + ksof(i + 4) * 1024, 16);
            }
            sb();
        }
    }
    STAMP(10);
    const int et = tid >> 3, c8 = tid & 7;
    const v4u xo = __builtin_amdgcn_raw_buffer_load_b128(rs_x, (64 * i2 + et) * 896 + c8 * 16, j2 * 128, 0);
    const v4u b2v = *(const v4u*)(p.b2 + 64 * j2 + 8 * c8);
#pragma unroll
    for (int m = 0; m < 4; m++)
#pragma unroll
        for (int i = 0; i < 4; i++)
            ldsw<f32x4>(lds, w * 16384 + (16 * m + r16) * 256 + (((4 * g + i) ^ r16) << 4), f32x4{oc[m][0][i], oc[m][1][i], oc[m][2][i], oc[m][3][i]});
    wait_flags(ws, WS_F3 + 32 * j2 * 8, 32, nonce, l);
    {
        const int pr = tid >> 4, u = tid & 15;
        const v4u at = __builtin_amdgcn_raw_buffer_load_b128(rs_ws, pr * 8192 + u * 16, WS_AP + (32 * j2) * 8192 + i2 * 256, 16);
        ldsw<v4u>(lds, LD_AT + pr * 256 + ((u ^ (pr >> 2)) << 4), at);
    }
    bar();
    STAMP(11);
    {
        f32x4 s0 = {0.f, 0.f, 0.f, 0.f}, s1 = {0.f, 0.f, 0.f, 0.f};
#pragma unroll
        for (int k = 0; k < 8; k++) {
            s0 += ldsr<f32x4>(lds, k * 16384 + et * 256 + (((2 * c8) ^ (et & 15)) << 4));
            s1 += ldsr<f32x4>(lds, k * 16384 + et * 256 + (((2 * c8 + 1) ^ (et & 15)) << 4));
        }
        v4u o;
#pragma unroll
        for (int e = 0; e < 4; e++) {
            const unsigned aa = ldsr<unsigned>(lds, LD_AT + (4 * c8 + e) * 256 + ((et ^ (c8 << 2)) << 2));
            const float ma = e < 2 ? s0[2 * e] : s1[2 * e - 4], mb = e < 2 ? s0[2 * e + 1] : s1[2 * e - 3];
            const unsigned mm = pkbf(ma, mb);
            const unsigned ml = pkbf(bflo(mm) + bflo(b2v[e]), bfhi(mm) + bfhi(b2v[e]));
            const unsigned xa = pkbf(bflo(xo[e]) + bflo(aa), bfhi(xo[e]) + bfhi(aa));
            o[e] = pkbf(bflo(xa) + bflo(ml), bfhi(xa) + bfhi(ml));
        }
        *(v4u*)(p.out + (64 * i2 + et) * D_ + 64 * j2 + 8 * c8) = o;
    }
    STAMP(12);
}

__global__ __launch_bounds__(512, 2) void fused_kernel(Params p) {
    extern __shared__ __attribute__((aligned(16))) char lds[];
    const int b = blockIdx.x;
    if (b < 32) aux_block(p, lds, b); else main_block(p, lds, b);
}

extern "C" void kernel_launch(void* const* d_in, const int* in_sizes, int n_in, void* d_out, int out_size, void* d_ws,
                              size_t ws_size, hipStream_t stream) {
    Params p;
    p.x = (const u16*)d_in[0]; p.are = (const u16*)d_in[1]; p.aim = (const u16*)d_in[2]; p.bre = (const u16*)d_in[3];
    p.bim = (const u16*)d_in[4]; p.cre = (const u16*)d_in[5]; p.cim = (const u16*)d_in[6]; p.logdt = (const u16*)d_in[7];
    p.dskip = (const u16*)d_in[8]; p.w1 = (const u16*)d_in[10]; p.b1 = (const u16*)d_in[11]; p.w2 = (const u16*)d_in[12];
    p.b2 = (const u16*)d_in[13]; p.out = (u16*)d_out; p.ws = (char*)d_ws;
    (void)hipFuncSetAttribute((const void*)fused_kernel, hipFuncAttributeMaxDynamicSharedMemorySize, LD_TOTAL);
    fused_kernel<<<256, 512, LD_TOTAL, stream>>>(p);
}
```

```cpp
#include <hip/hip_runtime.h>
#include <stdint.h>

typedef unsigned v4u __attribute__((ext_vector_type(4)));
typedef unsigned v2u __attribute__((ext_vector_type(2)));
typedef float f32x4 __attribute__((ext_vector_type(4)));
typedef float f32x2 __attribute__((ext_vector_type(2)));
typedef float f32x16 __attribute__((ext_vector_type(16)));
typedef __bf16 bf16x8 __attribute__((ext_vector_type(8)));
typedef _Float16 f16x8 __attribute__((ext_vector_type(8)));
typedef __bf16 bf16x2 __attribute__((ext_vector_type(2)));
typedef _Float16 f16x2 __attribute__((ext_vector_type(2)));
typedef short s16x4 __attribute__((ext_vector_type(4)));
typedef unsigned long long u64;
typedef unsigned short u16;
#define GAS __attribute__((address_space(1)))
#define LAS __attribute__((address_space(3)))
#define RLX_AGENT __ATOMIC_RELAXED, __HIP_MEMORY_SCOPE_AGENT
#define DEV __device__ __forceinline__

constexpr int L_ = 2048, D_ = 448, N_ = 256, F_ = 1792;
constexpr unsigned WS_E = 0, WS_FA = 1024, WS_FS = 1280, WS_FW = 12288, WS_F2 = 4096, WS_F3 = 8192, WS_ST = 16384;
constexpr unsigned XNT_S = 4096 + 256, AP_S = 8192 + 256;
constexpr unsigned WS_XNT = 1u << 20;
constexpr unsigned WS_W2T = WS_XNT + D_ * XNT_S;
constexpr unsigned WS_HF = WS_W2T + F_ * D_ * 2;
constexpr unsigned WS_AP = WS_HF + L_ * F_ * 2;
constexpr unsigned WS_END = WS_AP + 224 * AP_S;
constexpr int SCR_R = 288, SCR_W = 17 * SCR_R;
constexpr int LD_SCR = 98304;
constexpr int LD_U = 137472;
constexpr int LD_KT = 146688;
constexpr int LD_PAR = 149280;
constexpr int LD_CS = 157472;
constexpr int LD_B1 = 157984;
constexpr int LD_MISC = 158496;
constexpr int LD_CNT = LD_MISC + 144;
constexpr int LD_STAT = 158720;
constexpr int LD_CSP = 159744;
constexpr int LD_TOTAL = 161792;
constexpr int C_H = LD_CNT, C_A = LD_CNT + 4, C_F2 = LD_CNT + 8, C_CP = LD_CNT + 12, C_M = LD_CNT + 16, C_S = LD_CNT + 20  ,
              C_FA = LD_CNT + 28, C_G = LD_CNT + 32, C_FW = LD_CNT + 36, C_L = LD_CNT + 40, C_FS = LD_CNT + 44, C_CD = LD_CNT + 48,
              C_CS = LD_CNT + 52, C_F3 = LD_CNT + 56;
constexpr int LD_WST = LD_U + 512;
constexpr int TZ_CS = 4160, TZ_CH = 4 * SCR_W;
constexpr int LD_TZ = LD_SCR;
constexpr int LD_CP = 0;
constexpr int LD_AT = 146688;
constexpr unsigned SPIN_CAP = 1u << 17;
constexpr int HW = 5, SW = 8;
constexpr int CPF = 28;

struct Params {
    const u16 *x, *are, *aim, *bre, *bim, *cre, *cim, *logdt, *dskip, *w1, *b1, *w2, *b2;
    u16* out;
    char* ws;
};

DEV float bf2f(unsigned v) { return __uint_as_float(v << 16); }
DEV float bflo(unsigned v) { return __uint_as_float(v << 16); }
DEV float bfhi(unsigned v) { return __uint_as_float(v & 0xffff0000u); }
DEV unsigned pkbf(float a, float b) { f32x2 t = {a, b}; return __builtin_bit_cast(unsigned, __builtin_convertvector(t, bf16x2)); }
DEV unsigned pkh(float a, float b) { f32x2 t = {a, b}; return __builtin_bit_cast(unsigned, __builtin_convertvector(t, f16x2)); }
DEV float rbf(float a) { return bflo(pkbf(a, a)); }
DEV f32x4 mfma_bf(v4u a, v4u b, f32x4 c) { return __builtin_amdgcn_mfma_f32_16x16x32_bf16(__builtin_bit_cast(bf16x8, a), __builtin_bit_cast(bf16x8, b), c, 0, 0, 0); }
DEV f32x16 mfma32(v4u a, v4u b, f32x16 c) { return __builtin_amdgcn_mfma_f32_32x32x16_bf16(__builtin_bit_cast(bf16x8, a), __builtin_bit_cast(bf16x8, b), c, 0, 0, 0); }
DEV f32x4 mfma_h(v4u a, v4u b, f32x4 c) { return __builtin_amdgcn_mfma_f32_16x16x32_f16(__builtin_bit_cast(f16x8, a), __builtin_bit_cast(f16x8, b), c, 0, 0, 0); }
DEV float dot2bf(unsigned a, unsigned b, float c) { return __builtin_amdgcn_fdot2_f32_bf16(__builtin_bit_cast(bf16x2, a), __builtin_bit_cast(bf16x2, b), c, false); }
DEV __amdgpu_buffer_rsrc_t mkrs(const void* p, unsigned bytes) { return __builtin_amdgcn_make_buffer_rsrc((void*)p, 0, bytes, 0x00020000); }
DEV void st16_sc1(__amdgpu_buffer_rsrc_t rs, unsigned off, v4u v) {
    __builtin_amdgcn_raw_buffer_store_b128(v, rs, off, 0, 16);
    asm volatile("s_nop 1" ::: "memory");
}
DEV v4u ld16_sc1(__amdgpu_buffer_rsrc_t rs, unsigned vo, unsigned so) { return __builtin_amdgcn_raw_buffer_load_b128(rs, vo, so, 16); }
DEV void sb() { __builtin_amdgcn_sched_barrier(0); }
DEV void bar() { sb(); asm volatile("s_waitcnt lgkmcnt(0)" ::: "memory"); __builtin_amdgcn_s_barrier(); asm volatile("" ::: "memory"); sb(); }
template <int CTRL, int RM> DEV float dppadd(float x) { return x + __int_as_float(__builtin_amdgcn_update_dpp(0, __float_as_int(x), CTRL, RM, 0xf, true)); }
DEV float rowsum16(float v) { v = dppadd<0xB1, 0xf>(v); v = dppadd<0x4E, 0xf>(v); v = dppadd<0x124, 0xf>(v); v = dppadd<0x128, 0xf>(v); return v; }
DEV float wavesum_s(float v) {
    v = rowsum16(v);
    return __int_as_float(__builtin_amdgcn_readlane(__float_as_int(v), 0)) + __int_as_float(__builtin_amdgcn_readlane(__float_as_int(v), 16)) +
           __int_as_float(__builtin_amdgcn_readlane(__float_as_int(v), 32)) + __int_as_float(__builtin_amdgcn_readlane(__float_as_int(v), 48));
}
template <class T> DEV T ldsr(const char* lds, int off) { return *(const T*)(lds + off); }
template <class T> DEV void ldsw(char* lds, int off, T v) { *(T*)(lds + off) = v; }
DEV v4u ldtr(const char* lds, int off) {
    s16x4 lo = __builtin_amdgcn_ds_read_tr16_b64_v4i16((LAS s16x4*)(LAS char*)(lds + off));
    s16x4 hi = __builtin_amdgcn_ds_read_tr16_b64_v4i16((LAS s16x4*)(LAS char*)(lds + off + 1024));
    v2u a = __builtin_bit_cast(v2u, lo), b = __builtin_bit_cast(v2u, hi);
    return v4u{a[0], a[1], b[0], b[1]};
}
DEV u64 ldflag(const char* ws, unsigned off) { return __hip_atomic_load((const GAS u64*)(ws + off), RLX_AGENT); }
DEV u64 rmwflag(const char* ws, unsigned off) {
    u64 v;
    asm volatile("global_atomic_add_x2 %0, %1, %2, %3 sc0 sc1\n\ts_waitcnt vmcnt(0)" : "=&v"(v) : "v"(off), "v"(0ull), "s"(ws) : "memory");
    return v;
}
DEV void wait_flags(const char* ws, unsigned off, u64 v, u64 mask, u64 nonce) {
    if (__builtin_expect((__ballot(v != nonce) & mask) != 0, 0)) {
#pragma unroll 1
        for (unsigned spins = 0; spins < SPIN_CAP; ++spins) {
            __builtin_amdgcn_s_sleep(1);
            v = (spins & 63) == 63 ? rmwflag(ws, off) : ldflag(ws, off);
            if ((__ballot(v != nonce) & mask) == 0) break;
        }
    }
    asm volatile("" ::: "memory");
}
DEV void set_flag(char* ws, unsigned off, u64 nonce) { __hip_atomic_store((GAS u64*)(ws + off), nonce, RLX_AGENT); }


DEV void dma16(__amdgpu_buffer_rsrc_t rs, unsigned vo, unsigned so, unsigned ldsbase) {
    asm volatile("s_mov_b32 m0, %0\n\ts_nop 0\n\tbuffer_load_dwordx4 %1, %2, %3 offen lds" :: "s"(ldsbase), "v"(vo), "s"(rs), "s"(so) : "memory");
}
DEV void ald16o(v4u& d, __amdgpu_buffer_rsrc_t rs, unsigned vo, unsigned so, int hi) {
    if (hi) asm volatile("buffer_load_dwordx4 %0, %1, %2, %3 offen offset:1024 sc1" : "=v"(d) : "v"(vo), "s"(rs), "s"(so) : "memory");
    else asm volatile("buffer_load_dwordx4 %0, %1, %2, %3 offen sc1" : "=v"(d) : "v"(vo), "s"(rs), "s"(so) : "memory");
}
DEV int lane_id() { int x; asm volatile("v_mbcnt_lo_u32_b32 %0, -1, 0\n\tv_mbcnt_hi_u32_b32 %0, -1, %0" : "=v"(x)); return x; }
DEV void cpoll(u64& d, int skip, const char* ws, unsigned vo) {
    asm volatile("s_cmp_lg_u32 %3, 0\n\ts_cbranch_scc1 1f\n\tglobal_load_dwordx2 %0, %1, %2 sc1\n1:" : "+v"(d) : "v"(vo), "s"(ws), "s"(skip) : "memory", "scc");
}
#define WAITV(n) asm volatile("s_waitcnt vmcnt(%0)" :: "i"(n) : "memory")
DEV unsigned cvh(float a, float b) { unsigned r; asm("v_cvt_pk_f16_f32 %0, %1, %2" : "=v"(r) : "v"(a), "v"(b)); return r; }
DEV unsigned cvhc(float a, float b) { unsigned r; asm("v_cvt_pk_f16_f32 %0, %1, -%2" : "=v"(r) : "v"(a), "v"(b)); return r; }
DEV unsigned hmul(unsigned a, unsigned b) {
    unsigned t, r;
    asm("v_pk_mul_f16 %0, %1, %2 op_sel:[1,1] op_sel_hi:[1,0] neg_lo:[1,0]" : "=v"(t) : "v"(a), "v"(b));
    asm("v_pk_fma_f16 %0, %1, %2, %3 op_sel:[0,0,0] op_sel_hi:[0,1,1]" : "=v"(r) : "v"(a), "v"(b), "v"(t));
    return r;
}
struct cf { float x, y; };
DEV cf cmul(cf a, cf b) {
    cf r;
    asm("v_mul_f32 %0, %1, %2" : "=v"(r.x) : "v"(a.y), "v"(b.y));
    asm("v_mul_f32 %0, %1, %2" : "=v"(r.y) : "v"(a.y), "v"(b.x));
    asm("v_fma_f32 %0, %1, %2, -%0" : "+v"(r.x) : "v"(a.x), "v"(b.x));
    asm("v_fmac_f32 %0, %1, %2" : "+v"(r.y) : "v"(a.x), "v"(b.y));
    return r;
}
DEV cf cexp_l(float zr2, float zrev, float l) {
    float e = __builtin_amdgcn_exp2f(zr2 * l);
    float r = __builtin_amdgcn_fractf(zrev * l);
    return {e * __builtin_amdgcn_cosf(r), e * __builtin_amdgcn_sinf(r)};
}

DEV void aux_block(const Params& p, char* lds, int a, int w, u64 pc) {
    const int l = lane_id(), tid = w * 64 + l;
    const int r32 = l & 31, hh = l >> 5;
    char* ws = p.ws;
    const auto rs_x = mkrs(p.x, L_ * D_ * 2);
    const auto rs_ws = mkrs(ws, WS_END);
    v4u xv[2][2];
    const int row0 = 64 * a + 4 * w + 2 * hh;
    {
        const unsigned vo = row0 * 896 + r32 * 16, vo1 = r32 < 24 ? vo : 0x80000000u;
#pragma unroll
        for (int k = 0; k < 2; k++)
#pragma unroll
            for (int i = 0; i < 2; i++)
                asm volatile("buffer_load_dwordx4 %0, %1, %2, 0 offen offset:%3" : "=v"(xv[k][i]) : "v"(i ? vo1 : vo), "s"(rs_x), "i"(k * 896 + i * 512) : "memory");
    }
    u64 ev;
    asm volatile("global_load_dwordx2 %0, %1, %2 sc1" : "=v"(ev) : "v"(0), "s"(ws) : "memory");
    unsigned cv = 0;
    {
        asm volatile("global_load_dword %0, %1, %2" : "=v"(cv) : "v"(min(((a >> 3) * 16 + w) * 1024 + (l & 7) * 128, CPF * 1024 - 128)), "s"(pc) : "memory");
    }
    WAITV(1);
    asm volatile("" : "+v"(xv[0][0]), "+v"(xv[0][1]), "+v"(xv[1][0]), "+v"(xv[1][1]), "+v"(ev) :: "memory");
    const u64 nonce = ev + 1;
    float sc[2], of[2];
    {
        float s[2], q[2];
#pragma unroll
        for (int k = 0; k < 2; k++) {
            s[k] = 0.f; q[k] = 0.f;
#pragma unroll
            for (int i = 0; i < 2; i++)
#pragma unroll
                for (int c = 0; c < 4; c++) { s[k] = dot2bf(xv[k][i][c], 0x3F803F80u, s[k]); q[k] = dot2bf(xv[k][i][c], xv[k][i][c], q[k]); }
        }
        const bool odd = l & 1;
        float ss = (odd ? s[1] : s[0]) + __int_as_float(__builtin_amdgcn_update_dpp(0, __float_as_int(odd ? s[0] : s[1]), 0xB1, 0xf, 0xf, true));
        float qq = (odd ? q[1] : q[0]) + __int_as_float(__builtin_amdgcn_update_dpp(0, __float_as_int(odd ? q[0] : q[1]), 0xB1, 0xf, 0xf, true));
        ss = dppadd<0x4E, 0xf>(ss); ss = dppadd<0x124, 0xf>(ss); ss = dppadd<0x128, 0xf>(ss);
        qq = dppadd<0x4E, 0xf>(qq); qq = dppadd<0x124, 0xf>(qq); qq = dppadd<0x128, 0xf>(qq);
        { auto t = __builtin_amdgcn_permlane16_swap(__float_as_uint(ss), __float_as_uint(ss), false, false); ss = __uint_as_float(t[0]) + __uint_as_float(t[1]); }
        { auto t = __builtin_amdgcn_permlane16_swap(__float_as_uint(qq), __float_as_uint(qq), false, false); qq = __uint_as_float(t[0]) + __uint_as_float(t[1]); }
        const float mean = ss * (1.f / 448.f);
        const float var = fmaxf(qq * (1.f / 448.f) - mean * mean, 0.f);
        const float m1 = rbf(mean);
        const float s1 = rbf(__builtin_amdgcn_sqrtf(rbf(rbf(var) + 1e-5f)));
        const float r1 = __builtin_amdgcn_rcpf(s1);
        const float m2 = rbf((mean - m1) * r1);
        const float s2 = rbf(__builtin_amdgcn_sqrtf(rbf(rbf(var * r1 * r1) + 1e-5f)));
        const float Rs = r1 * __builtin_amdgcn_rcpf(s2), RM = Rs * (m1 + m2 * s1), o1 = -m1 * r1;
        if (r32 < 2) __hip_atomic_store((GAS u64*)(ws + WS_ST + (row0 + r32) * 8), (u64)__float_as_uint(Rs) | ((u64)__float_as_uint(RM) << 32), RLX_AGENT);
        sc[0] = __int_as_float(__builtin_amdgcn_mov_dpp(__float_as_int(r1), 0xA0, 0xf, 0xf, true)); sc[1] = __int_as_float(__builtin_amdgcn_mov_dpp(__float_as_int(r1), 0xF5, 0xf, 0xf, true));
        of[0] = __int_as_float(__builtin_amdgcn_mov_dpp(__float_as_int(o1), 0xA0, 0xf, 0xf, true)); of[1] = __int_as_float(__builtin_amdgcn_mov_dpp(__float_as_int(o1), 0xF5, 0xf, 0xf, true));
    }
    const int base = r32 * 8 * 128 + (((2 * w + hh) ^ ((r32 & 7) << 2)) << 2);
#pragma unroll
    for (int i = 0; i < 2; i++) {
        if (i == 1 && r32 >= 24) break;
#pragma unroll
        for (int c = 0; c < 4; c++) {
            const unsigned a0 = xv[0][i][c], a1 = xv[1][i][c];
            ldsw<unsigned>(lds, base + (i * 256 + 2 * c) * 128, pkh(__builtin_fmaf(bflo(a0), sc[0], of[0]), __builtin_fmaf(bflo(a1), sc[1], of[1])));
            ldsw<unsigned>(lds, base + (i * 256 + 2 * c + 1) * 128, pkh(__builtin_fmaf(bfhi(a0), sc[0], of[0]), __builtin_fmaf(bfhi(a1), sc[1], of[1])));
        }
    }
    asm volatile("s_waitcnt vmcnt(0)" : "+v"(cv) :: "memory");
    bar();
    if (tid == 0) set_flag(ws, WS_FS + a * 8, nonce);
    {
        const int u = tid & 7, d0 = tid >> 3;
        const int lo = d0 * 128 + ((u ^ ((d0 >> 3) & 7)) << 4);
        const unsigned go = WS_XNT + a * 128 + d0 * XNT_S + u * 16;
#pragma unroll
        for (int i = 0; i < 4; i++) if (i < 3 || w < 8) st16_sc1(rs_ws, go + i * 128 * XNT_S, ldsr<v4u>(lds, lo + i * 16384));
    }
    WAITV(0);
    bar();
    if (tid == 0) set_flag(ws, WS_FA + a * 8, nonce);
    if (a == 0 && w == 0) {
#pragma unroll 1
        for (unsigned spins = 0; spins < SPIN_CAP; ++spins) {
            bool ok = ldflag(ws, WS_FA + (l & 31) * 8) == nonce;
#pragma unroll
            for (int k = 0; k < 4; k++) { int idx = l + 64 * k; u64 v = ldflag(ws, WS_F2 + idx * 8); ok &= ((idx & 15) >= 14) || v == nonce; }
            if (__all(ok)) break;
            __builtin_amdgcn_s_sleep(32);
        }
        if (l == 0) set_flag(ws, WS_E, nonce);
    }
}

DEV void lds_spin(const char* lds, int off, unsigned want) {
#pragma unroll 1
    for (unsigned spins = 0; spins < (SPIN_CAP << 4); ++spins) {
        if (__builtin_amdgcn_readfirstlane(__hip_atomic_load((const unsigned*)(lds + off), __ATOMIC_RELAXED, __HIP_MEMORY_SCOPE_WORKGROUP)) >= want) break;
        __builtin_amdgcn_s_sleep(6);
    }
    asm volatile("" ::: "memory");
}
DEV unsigned lds_arrive(char* lds, int off, int l) {
    unsigned old = 0;
    if (l == 0) old = __hip_atomic_fetch_add((unsigned*)(lds + off), 1u, __ATOMIC_RELAXED, __HIP_MEMORY_SCOPE_WORKGROUP);
    return __builtin_amdgcn_readfirstlane(old);
}

DEV u64 uni64(u64 v) { return (u64)(unsigned)__builtin_amdgcn_readfirstlane((unsigned)v) | ((u64)(unsigned)__builtin_amdgcn_readfirstlane((unsigned)(v >> 32)) << 32); }
DEV void lds_add(char* lds, int off, int l) {
    asm volatile("" ::: "memory");
    if (l == 0) __hip_atomic_fetch_add((unsigned*)(lds + off), 1u, __ATOMIC_RELAXED, __HIP_MEMORY_SCOPE_WORKGROUP);
    asm volatile("" ::: "memory");
}
DEV void lds_release(char* lds, int off, int l) {
    asm volatile("s_waitcnt lgkmcnt(0)" ::: "memory");
    lds_add(lds, off, l);
}
DEV unsigned lds_peek(const char* lds, int off) { return __hip_atomic_load((const unsigned*)(lds + off), __ATOMIC_RELAXED, __HIP_MEMORY_SCOPE_WORKGROUP); }
DEV void lds_check(const char* lds, int off, unsigned v, unsigned want) {
    if (__builtin_amdgcn_readfirstlane(v) < want) {
#pragma unroll 1
        for (unsigned spins = 0; spins < (SPIN_CAP << 4); ++spins) {
            if (__builtin_amdgcn_readfirstlane(lds_peek(lds, off)) >= want) break;
            __builtin_amdgcn_s_sleep(1);
        }
    }
    asm volatile("" ::: "memory");
    sb();
}

DEV void main_block(const Params& p, char* lds, int b, int w) {
#define LANE_VARS const int l = lane_id(), tid = w * 64 + l, r16 = l & 15, g = l >> 4, r32 = l & 31, hh = l >> 5; (void)tid; (void)r16; (void)g; (void)r32; (void)hh; v16 = l * 16
    char* ws = p.ws;
    const int g8 = b & 7, rr = (b - 32) >> 3;
    const int i1 = 4 * (g8 >> 1) + rr / 7, j1 = 7 * (g8 & 1) + rr % 7;
    const int i2 = 4 * g8 + rr / 7, j2 = rr % 7;
    const int q = 28 * g8 + rr;
    const auto rs_x = mkrs(p.x, L_ * D_ * 2);
    const auto rs_ws = mkrs(ws, WS_END);
    const unsigned lbase = (unsigned)(uintptr_t)(LAS char*)lds;
    unsigned v16;
    v4u w2f[7][2];
    const unsigned w2lo = WS_W2T + (7 * w * 7 + j2) * 2048;
    const int rot = (j2 + 2 * (i2 & 3)) % 7;
    unsigned w2cur = w2lo + rot * 14336;
    auto w2l = [&](int i) {
        ald16o(w2f[i >> 1][i & 1], rs_ws, v16, w2cur, i & 1);
        if (i & 1) { w2cur += 14336; if (w2cur == w2lo + 7 * 14336) w2cur = w2lo; }
    };
    u64 nonce;
    if (w >= 8 && w < 12) {
        LANE_VARS;
        const int xw = w - 8;
        const auto rs_w1 = mkrs(p.w1, D_ * F_ * 2);
        const unsigned vx = (128 * i1 + 32 * xw + (l >> 3)) * 896 + (((l & 7) ^ g) << 4);
        const unsigned vw = (16 * xw + g) * 3584 + j1 * 256 + (((r16 >> 1) ^ (g << 1)) << 5) + (r16 & 1) * 16;
        auto dma = [&](int s) {
#pragma unroll
            for (int i = 0; i < 4; i++) {
                const unsigned lb = lbase + (s % 3) * 16384 + xw * 4096 + i * 1024;
                dma16(rs_x, (i & 1) ? vx ^ 64 : vx, s * 128 + i * 8 * 896, lb);
                dma16(rs_w1, vw, (64 * s + 4 * i) * 3584, lb + 49152);
            }
        };
        const unsigned selv = ((g ^ r16) & 1) ? 0u : 0x3F803F80u;
        const v4u sel = {selv, selv, selv, selv};
        f32x4 csa[4];
#pragma unroll
        for (int t = 0; t < 4; t++) csa[t] = f32x4{0.f, 0.f, 0.f, 0.f};
        const int ca0 = 49152 + (16 * xw + 8 * hh + (r16 >> 2)) * 256 + (l & 3) * 8, cx = ((g & 1) ^ ((r16 >> 2) << 1)) << 5;
        auto colsum = [&](int s) {
#pragma unroll
            for (int t = 0; t < 4; t++) csa[t] = mfma_bf(ldtr(lds, (s % 3) * 16384 + ca0 + (cx ^ (t << 6))), sel, csa[t]);
            sb();
        };
        u64 ev, pl = 0;
        asm volatile("global_load_dwordx2 %0, %1, %2 sc1" : "=v"(ev) : "v"(0), "s"(ws) : "memory");
        dma(0);
        bar();
        dma(1); dma(2);
        asm volatile("s_waitcnt vmcnt(16)" : "+v"(ev) :: "memory");
        nonce = uni64(ev + 1);
        if (tid == 512) ldsw<u64>(lds, LD_MISC, nonce);
        lds_add(lds, C_L, l);
        colsum(0);
        const unsigned fo = xw == 0 ? WS_FS + r32 * 8 : (xw == 1 ? WS_FW + (j2 * 64 + (l < 56 ? l : 0)) * 8 : WS_FA + r32 * 8);
#pragma unroll
        for (int s = 1; s < 7; s++) {
            if (s < 5) WAITV(8); else if (s == 5) WAITV(9); else WAITV(1);
            lds_add(lds, C_L, l);
            if (s + 2 < 7) { lds_spin(lds, C_M, 4 * s); dma(s + 2); }
            if (s == 4) cpoll(pl, 0, ws, fo);
            colsum(s);
        }
        if (r16 < 2) {
#pragma unroll
            for (int t = 0; t < 4; t++) ldsw<f32x4>(lds, LD_CSP + xw * 512 + (32 * t + 4 * g + 16 * r16) * 4, csa[t]);
        }
        if (lds_arrive(lds, C_CS, l) == 3) {
            const f32x2 a0 = ldsr<f32x2>(lds, LD_CSP + l * 8), a1 = ldsr<f32x2>(lds, LD_CSP + 512 + l * 8), a2 = ldsr<f32x2>(lds, LD_CSP + 1024 + l * 8), a3 = ldsr<f32x2>(lds, LD_CSP + 1536 + l * 8);
            ldsw<f32x2>(lds, LD_CS + l * 8, (a0 + a1) + (a2 + a3));
            lds_add(lds, C_CS, l);
        }
        asm volatile("s_waitcnt vmcnt(0)" : "+v"(pl) :: "memory");
        if (xw < 3) {
            __builtin_amdgcn_s_setprio(3);
            wait_flags(ws, fo, pl, ~0ull, nonce);
            if (xw == 0) ldsw<v4u>(lds, LD_STAT + l * 16, ld16_sc1(rs_ws, v16, WS_ST + i1 * 1024));
            if (xw == 2) {
                v4u uv[8];
#pragma unroll
                for (int i = 0; i < 8; i++) uv[i] = ld16_sc1(rs_ws, v16 + (i & 3) * 1024, WS_XNT + (2 * q + (i >> 2)) * XNT_S);
#pragma unroll
                for (int i = 0; i < 8; i++) ldsw<v4u>(lds, LD_U + (i >> 2) * 4608 + 512 + (i & 3) * 1024 + l * 16, uv[i]);
            }
            lds_add(lds, xw == 0 ? C_FS : (xw == 1 ? C_FW : C_FA), l);
            __builtin_amdgcn_s_setprio(0);
        }
        lds_spin(lds, C_FW, 1);
#pragma unroll
        for (int i = 0; i < 14; i++) w2l(i);
        if (w == 11) {
            lds_spin(lds, C_H, 4);
            const unsigned f2o = WS_F2 + ((i2 >> 1) * 16 + r16) * 8;
            wait_flags(ws, f2o, ldflag(ws, f2o), 0x3fffull, nonce);
            if (l == 0) __hip_atomic_store((unsigned*)(lds + C_F2), 1u, __ATOMIC_RELAXED, __HIP_MEMORY_SCOPE_WORKGROUP);
        }
    } else if (w >= 12) {
        LANE_VARS;
        const int wm = (w >> 1) & 1, wn = w & 1;
        const int un = q + 224 * wn;
        const bool hasu = wm == 0 && un < 392;
        unsigned bv;
        asm volatile("global_load_dword %0, %1, %2" : "=v"(bv) : "v"(j1 * 256 + l * 4), "s"(p.b1) : "memory");
        if (hasu) {
            const auto rs_w2 = mkrs(p.w2, F_ * D_ * 2);
#pragma unroll
            for (int i = 0; i < 4; i++)
                dma16(rs_w2, (l >> 3) * 896 + (((l & 7) ^ ((g & 1) << 2)) << 4), (32 * (un / 7) + 8 * i) * 896 + (un % 7) * 128, lbase + LD_WST + wn * 4608 + i * 1024);
        }
        bar();
        asm volatile("s_waitcnt vmcnt(0)" : "+v"(bv) :: "memory");
        if (hasu) {
            const int qq = r16 >> 2, pp = l & 3;
            const int ra = LD_WST + wn * 4608 + (4 * (g >> 1) + qq) * 128 + (g & 1) * 32 + pp * 8;
            const unsigned ob = WS_W2T + ((2 * (un / 7)) * 7 + (un % 7)) * 2048 + l * 16;
#pragma unroll
            for (int s2 = 0; s2 < 2; s2++)
#pragma unroll
                for (int ct = 0; ct < 2; ct++)
                    st16_sc1(rs_ws, ob + s2 * 7 * 2048 + ct * 1024, ldtr(lds, ra + s2 * 2048 + ((ct ^ ((qq >> 1) & 1)) << 6)));
        }
        if (w == 14) ldsw<f32x2>(lds, LD_B1 + l * 8, f32x2{bflo(bv), bfhi(bv)});
        int xa[4], wa[2];
        {
            const int qq = r16 >> 2, pp = l & 3;
#pragma unroll
            for (int k = 0; k < 4; k++) xa[k] = (64 * wm + r32) * 128 + (((2 * k + hh) ^ ((r32 >> 1) & 7)) << 4);
#pragma unroll
            for (int t = 0; t < 2; t++) wa[t] = 49152 + (8 * hh + qq) * 256 + (((2 * (2 * wn + t) + (g & 1)) ^ (qq << 1)) << 5) + pp * 8;
            asm("" : "+v"(wa[0]), "+v"(wa[1]));
        }
        f32x16 acc[2][2];
#pragma unroll
        for (int i = 0; i < 16; i++) { acc[0][0][i] = 0.f; acc[0][1][i] = 0.f; acc[1][0][i] = 0.f; acc[1][1][i] = 0.f; }
        v4u xf[2][2], wf[2][2];
        v2u stv[2];
        unsigned pk = 0, pf = 0, pc = 0;
        auto step = [&](int n) {
            const int c = n & 1, s1 = (n + 2) >> 2, k1 = (n + 2) & 3, B = (s1 % 3) * 16384;
            if (n >= 0) { acc[0][0] = mfma32(wf[c][0], xf[c][0], acc[0][0]); acc[1][0] = mfma32(wf[c][0], xf[c][1], acc[1][0]); }
            sb();
            if (k1 == 0 && n < 26) lds_check(lds, C_L, pk, 4 * (s1 + 1));
            if (n < 26) wf[c][0] = ldtr(lds, B + wa[0] + k1 * 4096);
            if (n >= 0) acc[0][1] = mfma32(wf[c][1], xf[c][0], acc[0][1]);
            sb();
            if (n < 26) xf[c][0] = ldsr<v4u>(lds, B + xa[k1]);
            if (n >= 0) acc[1][1] = mfma32(wf[c][1], xf[c][1], acc[1][1]);
            sb();
            if (n < 26) { xf[c][1] = ldsr<v4u>(lds, B + xa[k1] + 4096); wf[c][1] = ldtr(lds, B + wa[1] + k1 * 4096); }
            if (k1 == 3) pk = lds_peek(lds, C_L);
            if (k1 == 3 && n < 26) lds_add(lds, C_M, l);
            if (n == 26) { pf = lds_peek(lds, C_FS); pc = lds_peek(lds, C_CS); }
            if (n == 7) {
                nonce = uni64(ldsr<u64>(lds, LD_MISC));
                WAITV(0);
                if (hasu && l == 0) set_flag(ws, WS_FW + ((un % 7) * 64 + un / 7) * 8, nonce);
            }
            sb();
        };
#pragma unroll
        for (int n = -2; n < 28; n++) { step(n); }
        {
        LANE_VARS;
        lds_check(lds, C_FS, pf, 1);
        stv[0] = ldsr<v2u>(lds, LD_STAT + (64 * wm + r32) * 8); stv[1] = ldsr<v2u>(lds, LD_STAT + (64 * wm + 32 + r32) * 8);
        lds_check(lds, C_CS, pc, 5);
        __builtin_amdgcn_s_setprio(3);
        {
            const f32x2 k1 = {-2.302208198f, -2.302208198f}, k2 = {-0.1029432397f, -0.1029432397f}, one = {1.f, 1.f};
            const unsigned hb = WS_HF + ((4 * i1 + 2 * wm) * 112 + 8 * j1 + 4 * wn) * 1024 + l * 16;
            const int fo0 = LD_B1 + (64 * wn + 4 * hh) * 4;
#pragma unroll
            for (int t = 0; t < 2; t++) {
#pragma unroll
                for (int s2 = 0; s2 < 2; s2++) {
                    f32x4 bb[2], cc[2];
#pragma unroll
                    for (int gm = 0; gm < 2; gm++) { bb[gm] = ldsr<f32x4>(lds, fo0 + t * 128 + (2 * s2 + gm) * 32); cc[gm] = ldsr<f32x4>(lds, fo0 - 512 + t * 128 + (2 * s2 + gm) * 32); }
#pragma unroll
                    for (int mt = 0; mt < 2; mt++) {
                        const float Rs = __uint_as_float(stv[mt][0]), nRM = -__uint_as_float(stv[mt][1]);
                        f32x2 a[4], e[4];
#pragma unroll
                        for (int i = 0; i < 4; i++) {
                            const f32x2 ac = {acc[mt][t][8 * s2 + 2 * i], acc[mt][t][8 * s2 + 2 * i + 1]};
                            const f32x2 b2_ = {bb[i >> 1][2 * (i & 1)], bb[i >> 1][2 * (i & 1) + 1]}, c2 = {cc[i >> 1][2 * (i & 1)], cc[i >> 1][2 * (i & 1) + 1]};
                            a[i] = __builtin_elementwise_fma(ac, f32x2{Rs, Rs}, __builtin_elementwise_fma(c2, f32x2{nRM, nRM}, b2_));
                        }
#pragma unroll
                        for (int i = 0; i < 4; i++) e[i] = a[i] * __builtin_elementwise_fma(a[i] * a[i], k2, k1);
#pragma unroll
                        for (int i = 0; i < 4; i++) { e[i][0] = __builtin_amdgcn_exp2f(e[i][0]); e[i][1] = __builtin_amdgcn_exp2f(e[i][1]); }
#pragma unroll
                        for (int i = 0; i < 4; i++) e[i] += one;
#pragma unroll
                        for (int i = 0; i < 4; i++) { e[i][0] = __builtin_amdgcn_rcpf(e[i][0]); e[i][1] = __builtin_amdgcn_rcpf(e[i][1]); }
#pragma unroll
                        for (int i = 0; i < 4; i++) a[i] *= e[i];
                        st16_sc1(rs_ws, hb + mt * 112 * 1024 + (2 * t + s2) * 1024, v4u{pkbf(a[0][0], a[0][1]), pkbf(a[1][0], a[1][1]), pkbf(a[2][0], a[2][1]), pkbf(a[3][0], a[3][1])});
                        sb();
                    }
                }
            }
        }
        __builtin_amdgcn_s_setprio(0);
        lds_add(lds, C_G, l);
        lds_spin(lds, C_FW, 1);
        w2l(0); w2l(1); w2l(2); w2l(3);
        WAITV(4);
        if (lds_arrive(lds, C_H, l) == 3 && l == 0) set_flag(ws, WS_F2 + (i1 * 16 + j1) * 8, nonce);
#pragma unroll
        for (int i = 4; i < 14; i++) w2l(i);
        }
    } else {
        LANE_VARS;
        if (tid < 16) ldsw<unsigned>(lds, LD_CNT + tid * 4, 0u);
        const int ch = w >> 2, dch = 2 * q + ch, t8 = tid & 255;
        char* scr = lds + LD_SCR + w * SCR_W;
        const int lpar = LD_PAR + w * 1024;
        const int cs_ = C_S + ch * 4;
        {
            const int a = l >> 3;
            u64 pp[6] = {(u64)(uintptr_t)p.are, (u64)(uintptr_t)p.aim, (u64)(uintptr_t)p.bre, (u64)(uintptr_t)p.bim, (u64)(uintptr_t)p.cre, (u64)(uintptr_t)p.cim};
            asm("" : "+s"(pp[0]), "+s"(pp[1]), "+s"(pp[2]), "+s"(pp[3]), "+s"(pp[4]), "+s"(pp[5]));
            u64 src = pp[5];
#pragma unroll
            for (int k = 0; k < 5; k++) src = a == k ? pp[k] : src;
            const unsigned off = dch * 512 + (w & 3) * 128 + (l & 7) * 16;
            if (a < 6)
                asm volatile("s_mov_b32 m0, %0\n\ts_nop 0\n\tglobal_load_lds_dwordx4 %1, off" :: "s"(lbase + lpar), "v"(src + off) : "memory");
        }
        bar();
        const unsigned ldt2 = *(const __attribute__((address_space(4))) unsigned*)(uintptr_t)(p.logdt + 2 * q);
        const unsigned dsk = *(const __attribute__((address_space(4))) unsigned*)(uintptr_t)(p.dskip + 2 * q);
        const unsigned b2p = *(const __attribute__((address_space(4))) unsigned*)(uintptr_t)(p.b2 + 2 * q);
        asm volatile("" :: "s"(ldt2), "s"(dsk), "s"(b2p));
        float dt;
        {
            const float xl = ch ? bfhi(ldt2) : bflo(ldt2);
            const float t = xl * 1.4426950408889634f;
            const float e = __builtin_fmaf(xl, 1.4426950408889634f, -t) + xl * 1.925963033500011e-8f;
            const float y = __builtin_amdgcn_exp2f(t);
            dt = rbf(__builtin_fmaf(y * e, 0.6931471805599453f, y));
        }
        const int rd = r16 * SCR_R + g * 16, wl = l * 4;
        if (t8 >= 224) ldsw<v4u>(lds, LD_U + ch * 4608 + (t8 - 224) * 16, v4u{0u, 0u, 0u, 0u});
        unsigned pat[2]; int tb[2];
#pragma unroll
        for (int o = 0; o < 2; o++) {
            const int t = t8 + o;
            const int rg = (t < 128) + (t < 192) + (t < 224);
            const unsigned p3 = (t & 1) ? 0x222u : 0x2222u, p21 = t < 192 ? 0x22244u : 0x244468u, p10 = t < 224 ? p21 : 0x02468ACEu;
            pat[o] = t < 128 ? p3 : p10;
            tb[o] = LD_KT + ch * 1296 + 256 * rg + ((max(2040 - 8 * t, 0) >> rg) << 1);
        }
        sb();
        WAITV(0);
        cf W, E1, E8, E256;
        {
            unsigned pr[6];
#pragma unroll
            for (int a = 0; a < 6; a++) pr[a] = ldsr<u16>(lds, lpar + a * 128 + l * 2);
            float zr = dt * bf2f(pr[0]);
            const float zi = dt * bf2f(pr[1]);
            const float br = bf2f(pr[2]), bi = bf2f(pr[3]), cr = dt * bf2f(pr[4]), ci = dt * bf2f(pr[5]);
            W = cmul(cf{cr, ci}, cf{br, bi});
            float shift = 0.f;
            if (__builtin_expect(__any(zr > 0.f), 0)) {
                float m = fmaxf(zr, 0.f);
                for (int o = 32; o > 0; o >>= 1) m = fmaxf(m, __shfl_xor(m, o));
                shift = m; zr -= shift;
            }
            const float zr2 = zr * 1.4426950408889634f, zrev = zi * 0.15915494309189535f;
            E8 = cexp_l(zr2, zrev, 8.f);
            const float wsum = wavesum_s(fabsf(W.x) + fabsf(W.y));
            const int we = min(max(__builtin_amdgcn_frexp_expf(wsum), -100), 100);
            if (l == 0) { f32x4 info = {__builtin_amdgcn_ldexpf(1.f, we - 15), wsum, shift, 0.f}; ldsw<f32x4>(lds, LD_MISC + 16 + w * 16, info); }
            W.x = __builtin_amdgcn_ldexpf(W.x, 15 - we); W.y = __builtin_amdgcn_ldexpf(W.y, 15 - we);
            E1 = cexp_l(zr2, zrev, 1.f); E256 = cexp_l(zr2, zrev, 256.f);
        }
        auto row = [&](int r, unsigned v) { ldsw<unsigned>(scr, r * SCR_R + wl, v); };
        v4u fl[2][4], fh[4];
        f32x4 T[2][2];
        auto rdfr = [&](v4u (&f)[4]) {
#pragma unroll
            for (int k = 0; k < 4; k++) f[k] = ldsr<v4u>(scr, rd + k * 64);
        };
        auto kmma = [&](int th) {
#pragma unroll
            for (int tl = 0; tl < 2; tl++) T[th][tl] = f32x4{0.f, 0.f, 0.f, 0.f};
#pragma unroll
            for (int k = 0; k < 4; k++) { T[th][0] = mfma_h(fh[k], fl[0][k], T[th][0]); T[th][1] = mfma_h(fh[k], fl[1][k], T[th][1]); }
        };
        {
            const cf P2 = cmul(E8, E8), P3 = cmul(P2, E8), Q1 = cmul(P2, P2), Q2 = cmul(Q1, Q1), Q3 = cmul(Q2, Q1), Q4 = cmul(Q2, Q2);
            const unsigned p1 = cvh(E8.x, E8.y), p2 = cvh(P2.x, P2.y), p3 = cvh(P3.x, P3.y);
            const unsigned qh[3] = {cvh(Q1.x, Q1.y), cvh(Q2.x, Q2.y), cvh(Q3.x, Q3.y)};
            row(0, 0x00003C00u); row(1, p1); row(2, p2); row(3, p3);
#pragma unroll
            for (int k = 0; k < 3; k++) { row(4 + 4 * k, qh[k]); row(5 + 4 * k, hmul(qh[k], p1)); row(6 + 4 * k, hmul(qh[k], p2)); row(7 + 4 * k, hmul(qh[k], p3)); }
            row(16, cvh(Q4.x, Q4.y));
            rdfr(fl[0]);
#pragma unroll
            for (int k = 0; k < 4; k++) fh[k] = ldsr<v4u>(scr, 16 * SCR_R + g * 16 + k * 64);
        }
        sb();
        cf G;
        {
            const cf E2 = cmul(E1, E1), H1 = cmul(W, E1), E4 = cmul(E2, E2), H2 = cmul(W, E2), H3 = cmul(H1, E2);
            const cf G1 = cmul(W, E256), G2 = cmul(G1, E256), G3 = cmul(G2, E256);
            G = cmul(G3, E256);
            const unsigned e2c = cvhc(E2.x, E2.y), e4c = cvhc(E4.x, E4.y);
            const unsigned h[4] = {cvhc(W.x, W.y), cvhc(H1.x, H1.y), cvhc(H2.x, H2.y), cvhc(H3.x, H3.y)};
#pragma unroll
            for (int i = 0; i < 4; i++) { row(i, h[i]); row(4 + i, hmul(h[i], e4c)); }
            const unsigned g1 = cvhc(G1.x, G1.y), g1b = hmul(g1, e2c), g2 = cvhc(G2.x, G2.y), g3 = cvhc(G3.x, G3.y);
            row(8, g1); row(9, g1b); row(10, hmul(g1, e4c)); row(11, hmul(g1b, e4c));
            row(12, g2); row(13, hmul(g2, e4c)); row(14, g3); row(15, hmul(g3, e4c));
        }
        sb();
#pragma unroll
        for (int k = 0; k < 4; k++)
#pragma unroll
            for (int c = 0; c < 4; c++) fl[1][k][c] = hmul(fl[0][k][c], fh[k][c]);
        sb();
        rdfr(fh);
#pragma unroll
        for (int i = 0; i < 5; i++) { row(i, cvhc(G.x, G.y)); if (i < 4) G = cmul(G, E256); }
        kmma(0);
        rdfr(fh);
        {
            const int mul = g < 2 ? 32 : (g == 2 ? 16 : 8);
            const int i0 = (g < 2 ? 16 * g : (g == 2 ? 1024 : 1536)) + mul * r16;
            const int i1_ = i0 + (g == 3 ? 256 : 8);
            kmma(1);
#pragma unroll
            for (int tl = 0; tl < 2; tl++) {
                ldsw<f32x2>(scr, i0 + tl * 16 * mul, f32x2{T[0][tl][0], T[0][tl][1]});
                ldsw<f32x2>(scr, i1_ + tl * 16 * mul, f32x2{T[0][tl][2], T[0][tl][3]});
            }
            if (l <= 16) {
#pragma unroll
                for (int tl = 0; tl < 2; tl++)
#pragma unroll
                    for (int e = 0; e < 4; e++) ldsw<float>(scr, (512 + 32 * e + 16 * tl) * 4 + r16 * 4 + g * 512, T[1][tl][e]);
            }
        }
        lds_add(lds, cs_, l);
        lds_check(lds, cs_, 0, 4);
        {
            f32x4 inf[4];
            float pv[3][4];
#pragma unroll
            for (int k = 0; k < 4; k++) {
                inf[k] = ldsr<f32x4>(lds, LD_MISC + 16 + (4 * ch + k) * 16);
#pragma unroll
                for (int e = 0; e < 3; e++) pv[e][k] = ldsr<float>(lds, LD_SCR + (4 * ch + k) * SCR_W + t8 * 4 + e * 1024);
            }
            float bound = inf[0][1] + inf[1][1] + inf[2][1] + inf[3][1];
            const bool grow = __builtin_amdgcn_readfirstlane(__float_as_int(inf[0][2] + inf[1][2] + inf[2][2] + inf[3][2])) != 0;
            if (__builtin_expect(grow, 0)) {
                bound = 0.f;
                for (int k = 0; k < 4; k++) bound += inf[k][1] * expf(inf[k][2] * 2048.f);
                for (int e = 0; e < 3; e++) {
                    const int idx = t8 + 256 * e;
                    const int r3 = idx < 256 ? 0 : min((idx >> 7) - 1, 3);
                    const float lb = (float)((idx - 128 * r3) << r3);
                    for (int k = 0; k < 4; k++) pv[e][k] *= expf(inf[k][2] * lb);
                }
            }
            const int be = min(max(__builtin_amdgcn_frexp_expf(fminf(bound, 1e38f)), -100), 110);
#pragma unroll
            for (int k = 0; k < 4; k++) inf[k][0] = __builtin_amdgcn_ldexpf(inf[k][0], 15 - be);
            if (t8 == 0) ldsw<float>(lds, LD_MISC + 8 + ch * 4, __builtin_amdgcn_ldexpf(1.f, be - 15));
#pragma unroll
            for (int e = 0; e < 3; e++) {
                const float sm = __builtin_fmaf(pv[e][0], inf[0][0], __builtin_fmaf(pv[e][1], inf[1][0], __builtin_fmaf(pv[e][2], inf[2][0], pv[e][3] * inf[3][0])));
                if (e < 2 || t8 <= 128) ldsw<_Float16>(lds, LD_KT + ch * 1296 + t8 * 2 + e * 512, (_Float16)sm);
            }
        }
        lds_release(lds, cs_, l);
        lds_check(lds, cs_, 0, 8);
        {
            unsigned kv[11];
#pragma unroll
            for (int e = 0; e < 11; e++) kv[e] = ldsr<u16>(lds, tb[e >> 3] + __builtin_amdgcn_ubfe(pat[e >> 3], 4 * (e & 7), 4));
            const unsigned zm = t8 == 255 ? 0u : ~0u;
#pragma unroll
            for (int e = 8; e < 11; e++) kv[e] &= zm;
            const int tzb = LD_TZ + ch * TZ_CH + t8 * 16;
#pragma unroll
            for (int s = 0; s < 4; s++)
                ldsw<v4u>(lds, tzb + s * TZ_CS, v4u{kv[s] | (kv[s + 1] << 16), kv[s + 2] | (kv[s + 3] << 16), kv[s + 4] | (kv[s + 5] << 16), kv[s + 6] | (kv[s + 7] << 16)});
            if (t8 < 8) ldsw<v4u>(lds, LD_TZ + ch * TZ_CH + (t8 >> 1) * TZ_CS + 4096 + (t8 & 1) * 16, v4u{0u, 0u, 0u, 0u});
        }
        lds_add(lds, cs_, l);
        lds_spin(lds, C_FW, 1);
        nonce = uni64(ldsr<u64>(lds, LD_MISC));
#pragma unroll
        for (int i = 0; i < SW; i++) w2l(i);
        lds_spin(lds, C_FA, 1);
        lds_spin(lds, cs_, 12);
        lds_spin(lds, C_G, 4);
        __builtin_amdgcn_s_setprio(2);
        {
            const int par = w & 1, hf_ = (w >> 1) & 1;
            const int abase = LD_TZ + ch * TZ_CH + (3 - (r16 & 3)) * TZ_CS + (2028 - 128 * hf_ - 4 * (r16 >> 2) + 8 * g) * 2 - par * 512 - (192 + 3072);
            const int bbase = LD_U + ch * 4608 + (256 + 16 * r16 - 128 * hf_ - 16 + 8 * g) * 2 - 192;
            auto rdA = [&](int off) {
                const v2u lo = *(const volatile LAS v2u*)(LAS char*)(lds + off), hi = *(const volatile LAS v2u*)(LAS char*)(lds + off + 8);
                return v4u{lo[0], lo[1], hi[0], hi[1]};
            };
            f32x4 ca[8];
            v4u A[4], B[8];
#pragma unroll
            for (int t = 0; t < 8; t++) ca[t] = f32x4{0.f, 0.f, 0.f, 0.f};
#pragma unroll
            for (int k = 0; k < 4; k++) { B[2 * k] = ldsr<v4u>(lds, bbase + 192 + (2 * k) * 512); A[3 - k] = rdA(abase + (192 + 3072) - (3 - k) * 1024); B[2 * k + 1] = ldsr<v4u>(lds, bbase + 192 + (2 * k + 1) * 512); }
            sb();
#pragma unroll
            for (int c = 0; c < 4; c++) {
#pragma unroll
                for (int k = 3; k >= 0; k--) {
#pragma unroll
                    for (int pp = 0; pp <= 7 - 2 * k; pp++) {
                        ca[2 * k + pp] = mfma_h(A[k], B[pp], ca[2 * k + pp]);
                        if (k == 0 && c < 3) { B[pp] = ldsr<v4u>(lds, bbase + 192 - (c + 1) * 64 + pp * 512); sb(); }
                    }
                    if (c < 3) A[k] = rdA(abase + (192 + 3072) - (c + 1) * 64 - k * 1024);
                    sb();
                }
            }
            lds_spin(lds, C_M, 28);
            const int pb = LD_CP + w * 8192 + (4 * r16 + (g ^ ((r16 >> 1) & 3))) * 16;
            if (par) ca[7] = f32x4{0.f, 0.f, 0.f, 0.f};
#pragma unroll
            for (int t = 0; t < 7; t++) ldsw<f32x4>(lds, pb + par * 1024 + t * 1024, ca[t]);
            ldsw<f32x4>(lds, pb + (par ? 0 : 7168), ca[7]);
        }
        lds_add(lds, C_CP, l);
        lds_spin(lds, C_CP, 8);
        {
            const int po = LD_CP + (tid & ~3) * 16 + (((tid & 3) ^ ((tid >> 3) & 3)) << 4);
            const f32x2 ki = ldsr<f32x2>(lds, LD_MISC + 8);
            float av[2][4];
#pragma unroll
            for (int c = 0; c < 2; c++) {
                const f32x4 p0 = ldsr<f32x4>(lds, po + c * 32768), p1 = ldsr<f32x4>(lds, po + c * 32768 + 8192), p2 = ldsr<f32x4>(lds, po + c * 32768 + 16384), p3 = ldsr<f32x4>(lds, po + c * 32768 + 24576);
                const v2u uu = ldsr<v2u>(lds, LD_U + c * 4608 + 512 + tid * 8);
                const float ds = c ? bfhi(dsk) : bflo(dsk);
                const unsigned uu0 = uu[0], uu1 = uu[1];
                const f16x2 ua = __builtin_bit_cast(f16x2, uu0), ub = __builtin_bit_cast(f16x2, uu1);
                const float uf[4] = {(float)ua[0], (float)ua[1], (float)ub[0], (float)ub[1]};
#pragma unroll
                for (int i = 0; i < 4; i++) av[c][i] = __builtin_fmaf(uf[i], ds, __builtin_fmaf((p0[i] + p1[i]) + (p2[i] + p3[i]), ki[c], c ? bfhi(b2p) : bflo(b2p)));
            }
            st16_sc1(rs_ws, tid * 16 + WS_AP + q * AP_S, v4u{pkbf(av[0][0], av[1][0]), pkbf(av[0][1], av[1][1]), pkbf(av[0][2], av[1][2]), pkbf(av[0][3], av[1][3])});
        }
        sb();
        lds_release(lds, C_CD, l);
#pragma unroll
        for (int i = SW; i < 14; i++) w2l(i);
        WAITV(14 - SW);
        if (lds_arrive(lds, C_A, l) == 7 && l == 0) set_flag(ws, WS_F3 + q * 8, nonce);
    }
    {
    LANE_VARS;
    lds_spin(lds, C_F2, 1);
    v4u hf[14];
    const unsigned hb2 = WS_HF + (2 * i2) * 112 * 1024;
    unsigned hcur = hb2 + (7 * w + rot) * 1024;
    auto hload = [&](int i) {
        hf[i] = ld16_sc1(rs_ws, l * 16 + (i >= 7 ? 112 * 1024 : 0), hcur);
        hcur += 1024; if (hcur == hb2 + (7 * w + 7) * 1024) hcur -= 7 * 1024;
    };
#pragma unroll
    for (int i = 0; i < HW; i++) hload(i);
    sb();
    WAITV(HW);
    asm volatile("" : "+v"(w2f[0][0]), "+v"(w2f[0][1]), "+v"(w2f[1][0]), "+v"(w2f[1][1]), "+v"(w2f[2][0]), "+v"(w2f[2][1]), "+v"(w2f[3][0]), "+v"(w2f[3][1]),
                      "+v"(w2f[4][0]), "+v"(w2f[4][1]), "+v"(w2f[5][0]), "+v"(w2f[5][1]), "+v"(w2f[6][0]), "+v"(w2f[6][1]));
    const int pw = w * 4096 + r32 * 128;
    f32x16 oc[2];
    auto pwrite = [&](int m) {
#pragma unroll
        for (int c = 0; c < 2; c++)
#pragma unroll
            for (int gp = 0; gp < 2; gp++)
                ldsw<v4u>(lds, m * 65536 + pw + (((4 * c + 2 * gp + hh) ^ (l & 7)) << 4),
                          v4u{pkbf(oc[c][8 * gp], oc[c][8 * gp + 1]), pkbf(oc[c][8 * gp + 2], oc[c][8 * gp + 3]), pkbf(oc[c][8 * gp + 4], oc[c][8 * gp + 5]), pkbf(oc[c][8 * gp + 6], oc[c][8 * gp + 7])});
    };
#pragma unroll
    for (int i = 0; i < 16; i++) { oc[0][i] = 0.f; oc[1][i] = 0.f; }
#pragma unroll
    for (int i = 0; i < 7; i++) {
        oc[0] = mfma32(w2f[i][0], hf[i], oc[0]);
        oc[1] = mfma32(w2f[i][1], hf[i], oc[1]);
        hload(HW + i);
        sb();
    }
    lds_spin(lds, C_CD, 8);
    pwrite(0);
    __builtin_amdgcn_s_setprio(0);
    sb();
#pragma unroll
    for (int i = 0; i < 16; i++) { oc[0][i] = 0.f; oc[1][i] = 0.f; }
#pragma unroll
    for (int i = 0; i < 7; i++) {
        oc[0] = mfma32(w2f[i][0], hf[7 + i], oc[0]);
        oc[1] = mfma32(w2f[i][1], hf[7 + i], oc[1]);
        if (HW + 7 + i < 14) hload(HW + 7 + i);
        sb();
    }
    const int tk = tid >> 4, c4 = tid & 15;
    const v2u xo = __builtin_amdgcn_raw_buffer_load_b64(rs_x, (64 * i2 + tk) * 896 + c4 * 8, j2 * 128, 0);
    if (w == 8) {
        const unsigned f3o = WS_F3 + (32 * j2 + r32) * 8;
        const u64 p3 = ldflag(ws, f3o);
        pwrite(1);
        wait_flags(ws, f3o, p3, ~0ull, nonce);
        lds_add(lds, C_F3, l);
    } else pwrite(1);
    if (w >= 8) {
        lds_check(lds, C_F3, lds_peek(lds, C_F3), 1);
        ldsw<v4u>(lds, LD_AT + (tk & 31) * 256 + ((c4 ^ (w & 7)) << 4), ld16_sc1(rs_ws, (tk & 31) * AP_S + c4 * 16, WS_AP + (32 * j2) * AP_S + i2 * 256));
    }
    bar();
    {
        const int ro = (tk >> 5) * 65536 + (tk & 31) * 128 + ((((c4 >> 3) * 4 + ((c4 >> 2) & 1) * 2 + (c4 & 1)) ^ (tk & 7)) << 4) + ((c4 >> 1) & 1) * 8;
        v2u v[16];
#pragma unroll
        for (int k = 0; k < 16; k++) v[k] = ldsr<v2u>(lds, ro + k * 4096);
        float sum[4];
#pragma unroll
        for (int e = 0; e < 2; e++) {
            const int pr = 2 * c4 + e;
            const unsigned aa = ldsr<unsigned>(lds, LD_AT + pr * 256 + ((tk ^ ((pr >> 2) << 2)) << 2));
            sum[2 * e] = bflo(xo[e]) + bflo(aa); sum[2 * e + 1] = bfhi(xo[e]) + bfhi(aa);
        }
        float t[4] = {0.f, 0.f, 0.f, 0.f};
        unsigned klo = 0x00003F80u, khi = 0x3F800000u;
        asm("" : "+s"(klo), "+s"(khi));
#pragma unroll
        for (int k = 0; k < 16; k++)
#pragma unroll
            for (int e = 0; e < 2; e++) { const unsigned pv = v[k][e]; t[2 * e] = dot2bf(pv, klo, t[2 * e]); t[2 * e + 1] = dot2bf(pv, khi, t[2 * e + 1]); }
        *(v2u*)(p.out + (64 * i2 + tk) * D_ + 64 * j2 + 4 * c4) = v2u{pkbf(sum[0] + t[0], sum[1] + t[1]), pkbf(sum[2] + t[2], sum[3] + t[3])};
    }
    }
}

__global__ __launch_bounds__(1024, 4) void fused_kernel(Params p) {
    extern __shared__ __attribute__((aligned(16))) char lds[];
    const int b = blockIdx.x;
    u64 pc;
    asm volatile("s_getpc_b64 %0" : "=s"(pc));
    pc &= ~255ull;
    const int wv = __builtin_amdgcn_readfirstlane(threadIdx.x >> 6);
    asm volatile("" :: "s"(p.x), "s"(p.are), "s"(p.cim), "s"(p.logdt), "s"(p.dskip), "s"(p.w1), "s"(p.b1), "s"(p.w2), "s"(p.b2), "s"(p.out), "s"(p.ws));
    if (b < 32) aux_block(p, lds, b, wv, pc); else main_block(p, lds, b, wv ^ 8);
}

extern "C" void kernel_launch(void* const* d_in, const int* in_sizes, int n_in, void* d_out, int out_size, void* d_ws,
                              size_t ws_size, hipStream_t stream) {
    Params p;
    p.x = (const u16*)d_in[0]; p.are = (const u16*)d_in[1]; p.aim = (const u16*)d_in[2]; p.bre = (const u16*)d_in[3];
    p.bim = (const u16*)d_in[4]; p.cre = (const u16*)d_in[5]; p.cim = (const u16*)d_in[6]; p.logdt = (const u16*)d_in[7];
    p.dskip = (const u16*)d_in[8]; p.w1 = (const u16*)d_in[10]; p.b1 = (const u16*)d_in[11]; p.w2 = (const u16*)d_in[12];
    p.b2 = (const u16*)d_in[13]; p.out = (u16*)d_out; p.ws = (char*)d_ws;
    (void)hipFuncSetAttribute((const void*)fused_kernel, hipFuncAttributeMaxDynamicSharedMemorySize, LD_TOTAL);
    fused_kernel<<<256, 1024, LD_TOTAL, stream>>>(p);
}
```
